```python
import torch

def _softmax(x, axis=-1):
    return torch.softmax(x, dim=axis)
def _gelu(x):
    return torch.nn.functional.gelu(x, approximate="tanh")
def _silu(x):
    return torch.nn.functional.silu(x)
def _sigmoid(x):
    return torch.sigmoid(x)
def _layernorm(x, axis=-1, eps=1e-5):
    m = x.mean(dim=axis, keepdim=True)
    v = x.var(dim=axis, keepdim=True, unbiased=False)
    return (x - m) / torch.sqrt(v + eps)
def _scatter_add(tgt, idx, src, axis=0):
    return tgt.clone().index_add_(axis, idx, src)
def _segment_sum(src, seg, n):
    out = torch.zeros((n,) + src.shape[1:], dtype=src.dtype)
    return out.index_add_(0, seg, src)
def _pad(x, pw):
    flat = tuple(v for pair in reversed(pw) for v in pair)
    return torch.nn.functional.pad(x, flat)

kernel_name = 'gated_mix__linear_attn_block__grouped_kv_mqa__prompt'

def setup_inputs():

    _w = (lambda a: (lambda t: t.to(torch.bfloat16) if t.is_floating_point() else t)(torch.as_tensor(a)))
    def _A_setup():
        B, S, D = 1, 2048, 512
        return {
            "x": _w((lambda *s: torch.randn(*s, dtype=torch.bfloat16) * 0.05)(B, S, D)),
            "wqkv": _w((lambda *s: torch.randn(*s, dtype=torch.bfloat16) * 1.6)(D, 3 * D)),
            "wo": _w((lambda *s: torch.randn(*s, dtype=torch.bfloat16) * 0.05)(D, D)),
        }
    _A_in = _A_setup()

    def _B_setup():
        B, S, D, H = 1, 2048, 512, 32
        Dh = D // H
        return {
            "x": _w((lambda *s: torch.randn(*s, dtype=torch.bfloat16) * 0.05)(B, S, D)),
            "wq": _w((lambda *s: torch.randn(*s, dtype=torch.bfloat16) * 1.6)(D, H * Dh)),
            "wk": _w((lambda *s: torch.randn(*s, dtype=torch.bfloat16) * 1.6)(D, Dh)),
            "wv": _w((lambda *s: torch.randn(*s, dtype=torch.bfloat16) * 0.05)(D, Dh)),
            "wo": _w((lambda *s: torch.randn(*s, dtype=torch.bfloat16) * 0.05)(H * Dh, D)),
        }
    _B_in = _B_setup()

    return {
        "A_x": _A_in["x"],
        "A_wqkv": _A_in["wqkv"],
        "A_wo": _A_in["wo"],
        "B_x": _B_in["x"],
        "B_wq": _B_in["wq"],
        "B_wk": _B_in["wk"],
        "B_wv": _B_in["wv"],
        "B_wo": _B_in["wo"],
        "gate_w": _w((lambda *s: torch.randn(*s, dtype=torch.bfloat16) * 0.05)(512) * 16.0),
    }

def reference(A_x, A_wqkv, A_wo, B_x, B_wq, B_wk, B_wv, B_wo, gate_w):
    def _A_ref(x, wqkv, wo):
        B, S, D = x.shape
        H = 32; Dh = D // H
        qkv = (lambda x, *d: x.permute(*d))((x @ wqkv).reshape(B, S, 3, H, Dh), 2, 0, 3, 1, 4)
        q, k, v = qkv[0], qkv[1], qkv[2]
        phi = lambda t: torch.where(t > 0, t + 1, torch.exp(t)) + 1e-6
        pq, pk = phi(q), phi(k)
        kv = torch.einsum("bhsd,bhse->bhde", pk, v)
        z = torch.einsum("bhsd->bhd", pk)
        num = torch.einsum("bhqd,bhde->bhqe", pq, kv)
        den = torch.einsum("bhqd,bhd->bhq", pq, z)[..., None] + 1e-6
        o = num / den
        return (lambda x, *d: x.permute(*d))(o, 0, 2, 1, 3).reshape(B, S, D) @ wo

    def _B_ref(x, wq, wk, wv, wo):
        B, S, D = x.shape
        H = 32
        Dh = D // H
        q = (lambda x, *d: x.permute(*d))((x @ wq).reshape(B, S, H, Dh), 0, 2, 1, 3)
        k = x @ wk
        v = x @ wv
        scores = torch.einsum("bhqd,bkd->bhqk", q, k) * (Dh ** -0.5)
        p = _softmax(scores, axis=-1)
        o = torch.einsum("bhqk,bkd->bhqd", p, v)
        return (lambda x, *d: x.permute(*d))(o, 0, 2, 1, 3).reshape(B, S, H * Dh) @ wo

    x = A_x
    g = _sigmoid((x * gate_w).sum(-1, keepdims=True))
    a = _A_ref(x, A_wqkv, A_wo)
    b = _B_ref(x, B_wq, B_wk, B_wv, B_wo)
    return g * a + (1 - g) * b
```

```cpp
#include <hip/hip_runtime.h>
#include <hip/hip_bf16.h>
typedef __hip_bfloat16 bf16;
static __device__ float b2f(bf16 x){return __bfloat162float(x);}
static __device__ float rb(float x){return __bfloat162float(__float2bfloat16(x));}
__global__ void gemm_naive(const bf16* A, const bf16* W, float* C, int M, int N, int K, int ldw, int ldc){
  int n = blockIdx.x*64 + threadIdx.x%64; int m = blockIdx.y*4 + threadIdx.x/64;
  if (n>=N||m>=M) return;
  float s=0; for(int k=0;k<K;k++) s += b2f(A[m*K+k])*b2f(W[k*ldw+n]);
  C[m*ldc+n]=rb(s);
}
__device__ float phi(float t){ return (t>0? t+1.f : __expf(t)) + 1e-6f; }
__global__ void kvsum(const float* qkv, float* kv, float* z){
  int h=blockIdx.x, d=threadIdx.x/16, e=threadIdx.x%16; float s=0, zz=0;
  for(int r=0;r<2048;r++){ float pk=rb(phi(qkv[r*1536+512+h*16+d])); s+=pk*qkv[r*1536+1024+h*16+e]; zz+=pk; }
  kv[h*256+d*16+e]=rb(s); if(e==0) z[h*16+d]=rb(zz);
}
__global__ void oa(const float* qkv, const float* kv, const float* z, bf16* o){
  int r=blockIdx.x; int h=threadIdx.x/16, e=threadIdx.x%16; float num=0, den=0;
  for(int d=0;d<16;d++){ float pq=rb(phi(qkv[r*1536+h*16+d])); num+=pq*kv[h*256+d*16+e]; den+=pq*z[h*16+d]; }
  o[r*512+h*16+e]=__float2bfloat16(num/(den+1e-6f));
}
__global__ void attn(const float* q, const float* k, const float* v, bf16* o){
  int idx=blockIdx.x*64+threadIdx.x; int r=idx/32, h=idx%32; float qq[16];
  #pragma unroll
  for(int d=0;d<16;d++) qq[d]=q[r*512+h*16+d]*0.25f;
  float m=-1e30f,l=0,acc[16];
  #pragma unroll
  for(int d=0;d<16;d++) acc[d]=0;
  for(int j=0;j<2048;j++){ float s=0;
    #pragma unroll
    for(int d=0;d<16;d++) s+=qq[d]*k[j*16+d];
    float mn=fmaxf(m,s); float a=__expf(m-mn), p=__expf(s-mn); l=l*a+p;
    #pragma unroll
    for(int d=0;d<16;d++) acc[d]=acc[d]*a+p*v[j*16+d];
    m=mn; }
  #pragma unroll
  for(int d=0;d<16;d++) o[r*512+h*16+d]=__float2bfloat16(acc[d]/l);
}
__global__ void fin(const bf16* x, const bf16* gw, const float* a, const float* b, bf16* out){
  int r=blockIdx.x; float s=0; for(int k=0;k<512;k++) s+=b2f(x[r*512+k])*b2f(gw[k]);
  float g=1.f/(1.f+__expf(-s));
  out[r*512+threadIdx.x]=__float2bfloat16(g*a[r*512+threadIdx.x]+(1-g)*b[r*512+threadIdx.x]);
}
extern "C" void kernel_launch(void* const* d_in, const int* in_sizes, int n_in, void* d_out, int out_size, void* d_ws, size_t ws_size, hipStream_t stream){
  const bf16* x=(const bf16*)d_in[0]; const bf16* wqkv=(const bf16*)d_in[1]; const bf16* woA=(const bf16*)d_in[2];
  const bf16* wq=(const bf16*)d_in[4]; const bf16* wk=(const bf16*)d_in[5]; const bf16* wv=(const bf16*)d_in[6]; const bf16* woB=(const bf16*)d_in[7]; const bf16* gw=(const bf16*)d_in[8];
  float* ws=(float*)d_ws; float* qkv=ws; float* q=qkv+2048*1536; float* k=q+2048*512; float* v=k+2048*16; float* kv=v+2048*16; float* z=kv+32*256; float* a=z+512; float* b=a+2048*512;
  bf16* oA=(bf16*)(b+2048*512); bf16* oB=oA+2048*512;
  gemm_naive<<<dim3(24,512),256,0,stream>>>(x,wqkv,qkv,2048,1536,512,1536,1536);
  gemm_naive<<<dim3(8,512),256,0,stream>>>(x,wq,q,2048,512,512,512,512);
  gemm_naive<<<dim3(1,512),256,0,stream>>>(x,wk,k,2048,16,512,16,16);
  gemm_naive<<<dim3(1,512),256,0,stream>>>(x,wv,v,2048,16,512,16,16);
  kvsum<<<32,256,0,stream>>>(qkv,kv,z);
  oa<<<2048,512,0,stream>>>(qkv,kv,z,oA);
  attn<<<1024,64,0,stream>>>(q,k,v,oB);
  gemm_naive<<<dim3(8,512),256,0,stream>>>(oA,woA,a,2048,512,512,512,512);
  gemm_naive<<<dim3(8,512),256,0,stream>>>(oB,woB,b,2048,512,512,512,512);
  fin<<<2048,512,0,stream>>>(x,gw,a,b,(bf16*)d_out);
}
```

```cpp
#include <hip/hip_runtime.h>
#include <hip/hip_bf16.h>
#include <stdint.h>

#define LAS __attribute__((address_space(3)))
#define GAS __attribute__((address_space(1)))
typedef __attribute__((ext_vector_type(8))) __bf16 bf16x8;
typedef __attribute__((ext_vector_type(4))) __bf16 bf16x4;
typedef __attribute__((ext_vector_type(2))) __bf16 bf16x2;
typedef __attribute__((ext_vector_type(16))) __bf16 bf16x16;
typedef __attribute__((ext_vector_type(16))) float f32x16;
typedef __attribute__((ext_vector_type(8))) float f32x8;
typedef __attribute__((ext_vector_type(4))) float f32x4;
typedef __attribute__((ext_vector_type(2))) float f32x2;
typedef unsigned v4u __attribute__((ext_vector_type(4)));
typedef unsigned v2u __attribute__((ext_vector_type(2)));
typedef unsigned long long u64;
typedef GAS u64 gu64;

#define SB() __builtin_amdgcn_sched_barrier(0)
#define CFENCE() asm volatile("" ::: "memory")
#define RLX_AGENT __ATOMIC_RELAXED, __HIP_MEMORY_SCOPE_AGENT
#define RLX_WG __ATOMIC_RELAXED, __HIP_MEMORY_SCOPE_WORKGROUP

constexpr int XROW = 1040;
constexpr int L_X = 0;
constexpr int L_WQ = 66560;
constexpr int L_RING = 66560, RING_SZ = 14336;
constexpr int L_PQ = 123904;
constexpr int L_WOA = 0, L_WOB = 65536;
constexpr int L_WKV = 132096;
constexpr int L_OS = 132096, OS_SZ = 2560;
constexpr int L_Q = 152576;
constexpr int L_GW = 160768;
constexpr int L_G = 161792;
constexpr int L_CNT = 162048;
constexpr int L_DUMP = 162304;
constexpr int L_TOTAL = 163840;

constexpr int W_E = 0;
constexpr int W_F1 = 4096, W_F2 = 8192, W_F3 = 12288, W_F4 = 16384, W_F5 = 20480;
constexpr int W_K = 65536;
constexpr int W_V = 131072;
constexpr int W_KVS = 196608;
constexpr int W_KVP = 1 << 20;
constexpr int W_OA = 4 << 20;
constexpr int W_OB = 6 << 20;
constexpr int W_END = 8 << 20;
constexpr int W_DBG = 16 << 20;

constexpr unsigned SPIN_CAP = 1u << 18;

struct Params {
    const char* x; const char* wqkv; const char* woa; const char* wq; const char* wk; const char* wv;
    const char* wob; const char* gw; char* out; char* ws;
};

#define STAMP(i) do {} while (0)

__device__ __forceinline__ void glds16(unsigned voff, const char* base, unsigned lds) {
    asm volatile("s_mov_b32 m0, %2\n\ts_nop 0\n\tglobal_load_lds_dwordx4 %0, %1" :: "v"(voff), "s"(base), "s"(lds) : "memory");
}
__device__ __forceinline__ void glds4(unsigned voff, const char* base, unsigned lds) {
    asm volatile("s_mov_b32 m0, %2\n\ts_nop 0\n\tglobal_load_lds_dword %0, %1" :: "v"(voff), "s"(base), "s"(lds) : "memory");
}
template <int N> __device__ __forceinline__ void wait_vm() { asm volatile("s_waitcnt vmcnt(%0)" :: "n"(N) : "memory"); }
__device__ __forceinline__ void wait_lgkm0() { asm volatile("s_waitcnt lgkmcnt(0)" ::: "memory"); }
__device__ __forceinline__ void bar() { asm volatile("s_waitcnt lgkmcnt(0)\n\ts_barrier" ::: "memory"); }

__device__ __forceinline__ unsigned pk2(float a, float b) { bf16x2 t = {(__bf16)a, (__bf16)b}; return __builtin_bit_cast(unsigned, t); }
__device__ __forceinline__ bf16x8 pack8(float a0, float a1, float a2, float a3, float a4, float a5, float a6, float a7) {
    v4u v = {pk2(a0, a1), pk2(a2, a3), pk2(a4, a5), pk2(a6, a7)};
    return __builtin_bit_cast(bf16x8, v);
}
__device__ __forceinline__ bf16x8 ld128(LAS char* s, int off) { return *(LAS bf16x8*)(s + off); }
__device__ __forceinline__ bf16x8 ldtr(LAS char* s, int off0, int off1) {
    bf16x4 a = __builtin_amdgcn_ds_read_tr16_b64_v4bf16((LAS bf16x4*)(s + off0));
    bf16x4 b = __builtin_amdgcn_ds_read_tr16_b64_v4bf16((LAS bf16x4*)(s + off1));
    return __builtin_shufflevector(a, b, 0, 1, 2, 3, 4, 5, 6, 7);
}
__device__ __forceinline__ f32x16 mfma32(const bf16x8& a, const bf16x8& b, const f32x16& c) {
    return __builtin_amdgcn_mfma_f32_32x32x16_bf16(a, b, c, 0, 0, 0);
}
__device__ __forceinline__ bf16x16 lo8(const f32x16& s) { f32x8 t = __builtin_shufflevector(s, s, 0, 1, 2, 3, 4, 5, 6, 7); return __builtin_bit_cast(bf16x16, t); }
__device__ __forceinline__ bf16x16 hi8(const f32x16& s) { f32x8 t = __builtin_shufflevector(s, s, 8, 9, 10, 11, 12, 13, 14, 15); return __builtin_bit_cast(bf16x16, t); }
__device__ __forceinline__ f32x16 pv(const bf16x8& v, const bf16x16& pr, const f32x16& o, int idx) {
    return __builtin_amdgcn_smfmac_f32_32x32x32_bf16(v, pr, o, idx, 0, 0);
}
__device__ __forceinline__ float phi(float t) {
    return fmaxf(t, 0.f) + __builtin_amdgcn_exp2f(fminf(t, 0.f) * 1.44269504088896341f) + 1e-6f;
}

__device__ __forceinline__ void poll_flags(const char* base, int n, u64 tag, int l) {
    const gu64* f = (const gu64*)base + (l < n ? l : 0);
#pragma unroll 1
    for (unsigned s = 0; s < SPIN_CAP; ++s) {
        u64 v = __hip_atomic_load(f, RLX_AGENT);
        if (__builtin_amdgcn_ballot_w64(v != tag) == 0) break;
        __builtin_amdgcn_s_sleep(1);
    }
    CFENCE();
}
__device__ __forceinline__ void poll_lds(LAS unsigned* c, unsigned want) {
#pragma unroll 1
    for (unsigned s = 0; s < SPIN_CAP; ++s) {
        unsigned v = __hip_atomic_load(c, RLX_WG);
        if (__builtin_amdgcn_readfirstlane(v) == want) break;
        __builtin_amdgcn_s_sleep(2);
    }
    CFENCE();
}
__device__ __forceinline__ void arrive_and_flag(LAS unsigned* c, unsigned n, char* flag, u64 tag, int l) {
    asm volatile("s_waitcnt vmcnt(0) lgkmcnt(0)" ::: "memory");
    if (l == 0) {
        unsigned old = __hip_atomic_fetch_add(c, 1u, RLX_WG);
        if (old == n - 1) __hip_atomic_store((gu64*)flag, tag, RLX_AGENT);
    }
    CFENCE();
}

#define EX(S, i) S[i] = __builtin_amdgcn_exp2f(S[i])
__device__ __forceinline__ void step(f32x16& cur, f32x16& oth, f32x16& Oprev, f32x16& O, const bf16x8& Vprevhi,
                                     const bf16x8& Vlo, const bf16x8& Knext, const bf16x8& Qn, int idx) {
    EX(cur, 0); EX(cur, 1); SB();
    Oprev = pv(Vprevhi, hi8(oth), Oprev, idx); SB();
    EX(cur, 2); EX(cur, 3); EX(cur, 4); SB();
    { f32x16 z = {0}; oth = mfma32(Knext, Qn, z); } SB();
    EX(cur, 5); EX(cur, 6); EX(cur, 7); SB(); EX(cur, 8); EX(cur, 9); SB();
    O = pv(Vlo, lo8(cur), O, idx); SB();
    EX(cur, 10); EX(cur, 11); EX(cur, 12); EX(cur, 13); EX(cur, 14); EX(cur, 15); SB();
}
__device__ __forceinline__ void oput(const f32x16& O, LAS char* s, int l, unsigned ver) {
    *(volatile LAS f32x4*)(s + l * 16) = __builtin_shufflevector(O, O, 0, 1, 2, 3);
    *(volatile LAS f32x4*)(s + 1024 + l * 16) = __builtin_shufflevector(O, O, 4, 5, 6, 7);
    f32x2 t = {O[8], __uint_as_float(ver)};
    *(volatile LAS f32x2*)(s + 2048 + l * 8) = t;
}
__device__ __forceinline__ void oget(f32x16& O, LAS char* s, int l) {
    f32x2 t = *(volatile LAS f32x2*)(s + 2048 + l * 8);
    f32x4 a = *(volatile LAS f32x4*)(s + l * 16);
    f32x4 b = *(volatile LAS f32x4*)(s + 1024 + l * 16);
    O[0] = a[0]; O[1] = a[1]; O[2] = a[2]; O[3] = a[3]; O[4] = b[0]; O[5] = b[1]; O[6] = b[2]; O[7] = b[3];
    O[8] = t[0]; O[9] = t[1];
}
__device__ __forceinline__ void ocheck(f32x16& O, LAS char* s, int l, unsigned ver) {
    if (__builtin_expect(__builtin_amdgcn_ballot_w64(__float_as_uint(O[9]) < ver) != 0, 0)) {
#pragma unroll 1
        for (unsigned sp = 0; sp < SPIN_CAP; ++sp) {
            __builtin_amdgcn_s_sleep(2);
            oget(O, s, l);
            if (__builtin_amdgcn_ballot_w64(__float_as_uint(O[9]) < ver) == 0) break;
        }
    }
}
__device__ __forceinline__ void oaddput(f32x16& O, LAS char* s, int l, unsigned ver) {
    f32x16 T; oget(T, s, l); ocheck(T, s, l, ver);
#pragma unroll
    for (int r = 0; r < 9; ++r) T[r] += O[r];
    oput(T, s, l, ver + 1);
}

__device__ __forceinline__ void load_kv(bf16x8 (&Kf)[8], bf16x8 (&Vf)[16], __amdgpu_buffer_rsrc_t rs, int kb, int l) {
    const unsigned one = ((l & 27) == 16) ? 0x3f803f80u : 0u;
    v4u Vr[16];
#pragma unroll
    for (int i = 0; i < 16; ++i) Vr[i] = (v4u){one, one, one, one};
    const int ko = W_K + kb * 8192 + l * 16, vo = W_V + kb * 8192 + (l >> 5) * 256 + (l & 15) * 16;
#pragma unroll
    for (int i = 0; i < 8; ++i) Kf[i] = __builtin_bit_cast(bf16x8, __builtin_amdgcn_raw_buffer_load_b128(rs, ko + i * 1024, 0, 16));
    if ((l & 16) == 0) {
#pragma unroll
        for (int i = 0; i < 16; ++i) Vr[i] = __builtin_amdgcn_raw_buffer_load_b128(rs, vo + i * 512, 0, 16);
    }
#pragma unroll
    for (int i = 0; i < 16; ++i) Vf[i] = __builtin_bit_cast(bf16x8, Vr[i]);
}

__device__ __forceinline__ void attention(const bf16x8 (&Kf)[8], const bf16x8 (&Vf)[16], LAS char* sm, int pos, unsigned vb,
                                          int l, f32x16& O0, f32x16& O1) {
    int idx = 0xDDDD; asm volatile("" : "+v"(idx));
    LAS char* Os = sm + L_OS;
    LAS char* Qs = sm + L_Q + l * 16;
    f32x16 A, B;
    O0 = (f32x16){0}; O1 = (f32x16){0};
    int ce = (2 * pos) & 7;
    bf16x8 Q = ld128(Qs, ce * 1024), Q1 = ld128(Qs, (ce + 1) * 1024);
    A = mfma32(Kf[0], Q, O0);
    {
        EX(A, 0); EX(A, 1); EX(A, 2); EX(A, 3); EX(A, 4); SB();
        { f32x16 z = {0}; B = mfma32(Kf[1], Q, z); } SB();
        EX(A, 5); EX(A, 6); EX(A, 7); SB(); EX(A, 8); EX(A, 9); SB();
        O0 = pv(Vf[0], lo8(A), O0, idx); SB();
        EX(A, 10); EX(A, 11); EX(A, 12); EX(A, 13); EX(A, 14); EX(A, 15); SB();
        step(B, A, O0, O0, Vf[1], Vf[2], Kf[2], Q, idx);
        step(A, B, O0, O0, Vf[3], Vf[4], Kf[3], Q, idx);
        step(B, A, O0, O0, Vf[5], Vf[6], Kf[4], Q, idx);
        step(A, B, O0, O0, Vf[7], Vf[8], Kf[5], Q, idx);
        step(B, A, O0, O0, Vf[9], Vf[10], Kf[6], Q, idx);
        step(A, B, O0, O0, Vf[11], Vf[12], Kf[7], Q, idx);
        step(B, A, O0, O0, Vf[13], Vf[14], Kf[0], Q1, idx);
        Q = ld128(Qs, ((ce + 2) & 7) * 1024);
        step(A, B, O0, O1, Vf[15], Vf[0], Kf[1], Q1, idx);
        step(B, A, O1, O1, Vf[1], Vf[2], Kf[2], Q1, idx);
        oaddput(O0, Os + ce * OS_SZ, l, vb); SB();
        step(A, B, O1, O1, Vf[3], Vf[4], Kf[3], Q1, idx);
        step(B, A, O1, O1, Vf[5], Vf[6], Kf[4], Q1, idx);
        step(A, B, O1, O1, Vf[7], Vf[8], Kf[5], Q1, idx);
        step(B, A, O1, O1, Vf[9], Vf[10], Kf[6], Q1, idx);
        oget(O0, Os + ((ce + 2) & 7) * OS_SZ, l); SB();
        step(A, B, O1, O1, Vf[11], Vf[12], Kf[7], Q1, idx);
        ocheck(O0, Os + ((ce + 2) & 7) * OS_SZ, l, vb + 1); SB();
        step(B, A, O1, O1, Vf[13], Vf[14], Kf[0], Q, idx);
    }
#pragma unroll 1
    for (int cp = 1; cp < 4; ++cp) {
        const int cprev = ce + 1;
        ce = (ce + 2) & 7;
        Q1 = ld128(Qs, (ce + 1) * 1024);
        step(A, B, O1, O0, Vf[15], Vf[0], Kf[1], Q, idx);
        step(B, A, O0, O0, Vf[1], Vf[2], Kf[2], Q, idx);
        if (cp == 1) oaddput(O1, Os + cprev * OS_SZ, l, vb);
        else oput(O1, Os + cprev * OS_SZ, l, vb + cp);
        SB();
        step(A, B, O0, O0, Vf[3], Vf[4], Kf[3], Q, idx);
        step(B, A, O0, O0, Vf[5], Vf[6], Kf[4], Q, idx);
        step(A, B, O0, O0, Vf[7], Vf[8], Kf[5], Q, idx);
        step(B, A, O0, O0, Vf[9], Vf[10], Kf[6], Q, idx);
        oget(O1, Os + (ce + 1) * OS_SZ, l); SB();
        step(A, B, O0, O0, Vf[11], Vf[12], Kf[7], Q, idx);
        ocheck(O1, Os + (ce + 1) * OS_SZ, l, vb + cp); SB();
        step(B, A, O0, O0, Vf[13], Vf[14], Kf[0], Q1, idx);
        const int cn = cp == 3 ? ce : ((ce + 2) & 7);
        Q = ld128(Qs, cn * 1024);
        step(A, B, O0, O1, Vf[15], Vf[0], Kf[1], Q1, idx);
        step(B, A, O1, O1, Vf[1], Vf[2], Kf[2], Q1, idx);
        oput(O0, Os + ce * OS_SZ, l, vb + cp + 1); SB();
        step(A, B, O1, O1, Vf[3], Vf[4], Kf[3], Q1, idx);
        step(B, A, O1, O1, Vf[5], Vf[6], Kf[4], Q1, idx);
        step(A, B, O1, O1, Vf[7], Vf[8], Kf[5], Q1, idx);
        step(B, A, O1, O1, Vf[9], Vf[10], Kf[6], Q1, idx);
        oget(O0, Os + cn * OS_SZ, l); SB();
        step(A, B, O1, O1, Vf[11], Vf[12], Kf[7], Q1, idx);
        ocheck(O0, Os + cn * OS_SZ, l, vb + cp + 1); SB();
        step(B, A, O1, O1, Vf[13], Vf[14], Kf[0], Q, idx);
    }
    O1 = pv(Vf[15], hi8(B), O1, idx);
}

__device__ __forceinline__ void slow_tile(f32x16& O, const bf16x8& Q, __amdgpu_buffer_rsrc_t rs, int l) {
    const f32x16 z = {0};
    float m = -__builtin_inff();
#pragma unroll 1
    for (int t = 0; t < 64; ++t) {
        bf16x8 K = __builtin_bit_cast(bf16x8, __builtin_amdgcn_raw_buffer_load_b128(rs, W_K + t * 1024 + l * 16, 0, 16));
        f32x16 S = mfma32(K, Q, z);
#pragma unroll
        for (int r = 0; r < 16; ++r) m = fmaxf(m, S[r]);
    }
    m = fmaxf(m, __shfl_xor(m, 32));
    f32x16 C;
#pragma unroll
    for (int r = 0; r < 16; ++r) C[r] = -m;
    O = z;
    const unsigned one = ((l & 27) == 16) ? 0x3f803f80u : 0u;
#pragma unroll 1
    for (int t = 0; t < 64; ++t) {
        bf16x8 K = __builtin_bit_cast(bf16x8, __builtin_amdgcn_raw_buffer_load_b128(rs, W_K + t * 1024 + l * 16, 0, 16));
        v4u v0 = {one, one, one, one}, v1 = v0;
        if ((l & 16) == 0) {
            const int vo = W_V + t * 1024 + (l >> 5) * 256 + (l & 15) * 16;
            v0 = __builtin_amdgcn_raw_buffer_load_b128(rs, vo, 0, 16);
            v1 = __builtin_amdgcn_raw_buffer_load_b128(rs, vo + 512, 0, 16);
        }
        f32x16 S = mfma32(K, Q, C);
#pragma unroll
        for (int r = 0; r < 16; ++r) EX(S, r);
        O = pv(__builtin_bit_cast(bf16x8, v0), lo8(S), O, 0xDDDD);
        O = pv(__builtin_bit_cast(bf16x8, v1), hi8(S), O, 0xDDDD);
    }
}
__device__ __forceinline__ bool unsafe_sum(const f32x16& O) {
    float a = fabsf(O[0]);
#pragma unroll
    for (int r = 1; r < 9; ++r) a += fabsf(O[r]);
    return !(O[8] >= 0x1p-80f) || !(a < 0x1p100f);
}

__global__ __launch_bounds__(512, 2) void fused_kernel(Params p) {
    __shared__ __attribute__((aligned(1024))) char smem[L_TOTAL];
    LAS char* sm = (LAS char*)smem;
    const unsigned sm0 = (unsigned)(uintptr_t)sm;
    const int tid = threadIdx.x, l = tid & 63;
    const int w = __builtin_amdgcn_readfirstlane(tid >> 6);
    const int b = blockIdx.x, rk = b >> 3;
    const int qt = (b & 7) * 4 + (rk & 3), hg = rk >> 2;
    char* ws = p.ws;
    STAMP(0);
    const unsigned l16 = l * 16;
    const int r0 = (hg & 3) * 16;
    const char* xt = p.x + qt * 65536;
    {
        const char* wkv = (hg >> 2) ? p.wv : p.wk;
        glds16(l16, wkv + (2 * w) * 1024, sm0 + L_WKV + (2 * w) * 1024);
        glds16(l16, wkv + (2 * w + 1) * 1024, sm0 + L_WKV + (2 * w + 1) * 1024);
        glds16(l16, xt + (r0 + 2 * w) * 1024, sm0 + L_X + (r0 + 2 * w) * XROW);
        glds16(l16, xt + (r0 + 2 * w + 1) * 1024, sm0 + L_X + (r0 + 2 * w + 1) * XROW);
        if (w == 4) glds16(l16, p.gw, sm0 + L_GW);
    }
    u64 e0;
    asm volatile("s_load_dwordx2 %0, %1, 0x0\n\ts_waitcnt lgkmcnt(0)" : "=s"(e0) : "s"(ws + W_E) : "memory");
    const u64 tag = e0 * 6364136223846793005ull + 1442695040888963407ull;
    const __amdgpu_buffer_rsrc_t rs = __builtin_amdgcn_make_buffer_rsrc(ws, 0, W_END, 0x00020000);
    wait_vm<0>();
    bar();
    STAMP(1);
    const int g4 = l >> 4, q4 = (l >> 2) & 3, p4 = l & 3;
    const unsigned wsrc = (l >> 3) * 1024 + hg * 128 + (((l & 7) ^ ((l >> 2) & 4)) * 16);
    if (w != 4) {
        const int wi = w < 4 ? w : w - 1;
#pragma unroll
        for (int i = 0; i < 16; ++i) {
            const int id = i * 7 + wi;
            if (id < 48) { const int rr = (r0 + 16 + id) & 63; glds16(l16, xt + rr * 1024, sm0 + L_X + rr * XROW); }
            else { const int pc = (id - 48 + 2 * rk) & 63; glds16(wsrc, p.wq + pc * 8192, sm0 + L_WQ + pc * 1024); }
        }
    } else {
        f32x4 c0 = {0}, c1 = {0};
        LAS char* xa = sm + L_X + (r0 + (l & 15)) * XROW + g4 * 16;
        LAS char* wa = sm + L_WKV + (8 * g4 + q4) * 32 + p4 * 8;
        const int kg = qt * 64 + r0;
        int off;
        if (hg >> 2) {
#pragma unroll
            for (int s = 0; s < 16; s += 2) {
                c0 = __builtin_amdgcn_mfma_f32_16x16x32_bf16(ld128(xa, s * 64), ldtr(wa, s * 1024, s * 1024 + 128), c0, 0, 0, 0);
                c1 = __builtin_amdgcn_mfma_f32_16x16x32_bf16(ld128(xa, s * 64 + 64), ldtr(wa, s * 1024 + 1024, s * 1024 + 1152), c1, 0, 0, 0);
            }
            off = W_V + (kg >> 4) * 512 + (g4 >> 1) * 256 + (l & 15) * 16 + (g4 & 1) * 8;
        } else {
#pragma unroll
            for (int s = 0; s < 16; s += 2) {
                c0 = __builtin_amdgcn_mfma_f32_16x16x32_bf16(ldtr(wa, s * 1024, s * 1024 + 128), ld128(xa, s * 64), c0, 0, 0, 0);
                c1 = __builtin_amdgcn_mfma_f32_16x16x32_bf16(ldtr(wa, s * 1024 + 1024, s * 1024 + 1152), ld128(xa, s * 64 + 64), c1, 0, 0, 0);
            }
            off = W_K + (kg >> 5) * 1024 + (g4 & 1) * 512 + ((kg & 31) + (l & 15)) * 16 + (g4 >> 1) * 8;
        }
        c0 += c1;
        v2u pv2 = {pk2(c0[0], c0[1]), pk2(c0[2], c0[3])};
        __builtin_amdgcn_raw_buffer_store_b64(pv2, rs, off, 0, 16);
        const v4u z4 = {0u, 0u, 0u, 0u};
#pragma unroll
        for (int i = 0; i < 20; ++i) *(LAS v4u*)(sm + L_OS + i * 1024 + l16) = z4;
        if (l < 16) *(LAS v4u*)(sm + L_CNT + l16) = z4;
        wait_vm<0>();
        if (l == 0) __hip_atomic_store((gu64*)(ws + W_F1) + qt * 8 + hg, tag, RLX_AGENT);
        STAMP(2);
    }
    wait_vm<0>();
    bar();
    STAMP(3);
    LAS unsigned* cnt = (LAS unsigned*)(sm + L_CNT);
    const int rh = w & 1, hp = (w >> 1) & 1;
    if (w < 4) {
        LAS char* xa = sm + L_X + (32 * rh + (l & 31)) * XROW + (l >> 5) * 16;
        LAS char* wa = sm + L_WQ + (8 * (g4 >> 1) + q4) * 128 + (((4 * hp + 2 * (g4 & 1) + (p4 >> 1)) ^ ((q4 & 2) << 1)) * 16) + (p4 & 1) * 8;
        f32x16 c = {0};
#pragma unroll
        for (int s = 0; s < 32; ++s) c = mfma32(ldtr(wa, s * 2048, s * 2048 + 512), ld128(xa, s * 32), c);
        c *= 0.25f * 1.44269504088896341f;
        *(LAS bf16x8*)(sm + L_Q + (rh * 4 + 2 * hp) * 1024 + l16) = pack8(c[0], c[1], c[2], c[3], c[4], c[5], c[6], c[7]);
        *(LAS bf16x8*)(sm + L_Q + (rh * 4 + 2 * hp + 1) * 1024 + l16) = pack8(c[8], c[9], c[10], c[11], c[12], c[13], c[14], c[15]);
    } else {
        const int j = w - 4;
        {
            const int row = 16 * rk + 4 * j + g4;
            glds4(row * 3072 + (l & 15) * 128, p.wqkv, sm0 + L_DUMP + j * 256);
            glds4(row * 3072 + 2048 + (l & 7) * 128, p.wqkv, sm0 + L_DUMP + j * 256);
            glds4(row * 1024 + (l & 7) * 128, p.woa, sm0 + L_DUMP + j * 256);
            glds4(row * 1024 + (l & 7) * 128, p.wob, sm0 + L_DUMP + j * 256);
        }
        LAS char* xa = sm + L_X + (16 * j + (l & 15)) * XROW + g4 * 256;
        LAS char* ga = sm + L_GW + g4 * 256;
        float s0 = 0.f, s1 = 0.f;
#pragma unroll
        for (int i = 0; i < 16; ++i) {
            const bf16x8 xv = ld128(xa, i * 16), gv = ld128(ga, i * 16);
            s0 = __builtin_amdgcn_fdot2_f32_bf16(__builtin_shufflevector(xv, xv, 0, 1), __builtin_shufflevector(gv, gv, 0, 1), s0, false);
            s1 = __builtin_amdgcn_fdot2_f32_bf16(__builtin_shufflevector(xv, xv, 2, 3), __builtin_shufflevector(gv, gv, 2, 3), s1, false);
            s0 = __builtin_amdgcn_fdot2_f32_bf16(__builtin_shufflevector(xv, xv, 4, 5), __builtin_shufflevector(gv, gv, 4, 5), s0, false);
            s1 = __builtin_amdgcn_fdot2_f32_bf16(__builtin_shufflevector(xv, xv, 6, 7), __builtin_shufflevector(gv, gv, 6, 7), s1, false);
        }
        s0 += s1;
        s0 += __shfl_xor(s0, 16);
        s0 += __shfl_xor(s0, 32);
        if (l < 16) *(LAS float*)(sm + L_G + (16 * j + l) * 4) = __builtin_amdgcn_rcpf(1.f + __expf(-s0));
    }
    bar();
    STAMP(4);

    if (w < 4) {
        bf16x8 Kf[8], Vf[16];
        poll_flags(ws + W_F1 + w * 256, 32, tag, l);
        STAMP(5);
        load_kv(Kf, Vf, rs, w, l);
        __builtin_amdgcn_s_setprio(1);
        f32x16 O0, O1;
        attention(Kf, Vf, sm, w, 0u, l, O0, O1);
        oput(O1, sm + L_OS + ((2 * w + 7) & 7) * OS_SZ, l, 4u);
        __builtin_amdgcn_s_setprio(3);
        STAMP(6);
        poll_lds(cnt + 0, 4u);
        bf16x8 pq0 = ld128(sm, L_PQ + w * 2048 + l16), pq1 = ld128(sm, L_PQ + w * 2048 + 1024 + l16);
        const float g = *(LAS float*)(sm + L_G + (32 * rh + (l & 31)) * 4);
        asm volatile("s_waitcnt lgkmcnt(0)" : "+v"(pq0), "+v"(pq1) :: "memory");
#pragma unroll
        for (int i = 0; i < 16; ++i) glds16(wsrc, p.woa + (i * 4 + w) * 8192, sm0 + L_WOA + (i * 4 + w) * 1024);
#pragma unroll
        for (int i = 0; i < 14; ++i) glds16(wsrc, p.wob + (i * 4 + w) * 8192, sm0 + L_WOB + (i * 4 + w) * 1024);
        {
            const int pa = 57 + 2 * w, pb = w == 3 ? 56 : 58 + 2 * w;
            glds16(wsrc, p.wob + pa * 8192, sm0 + L_WOB + pa * 1024);
            glds16(wsrc, p.wob + pb * 8192, sm0 + L_WOB + pb * 1024);
        }
        poll_flags(ws + W_F3 + hg * 256, 32, tag, l);
        STAMP(7);
        {
#pragma unroll
            for (int a = 0; a < 2; ++a) {
                const int off = W_KVS + hg * 8192 + (2 * hp + a) * 2048 + l16;
                f32x4 u = __builtin_bit_cast(f32x4, __builtin_amdgcn_raw_buffer_load_b128(rs, off, 0, 16));
                f32x4 v = __builtin_bit_cast(f32x4, __builtin_amdgcn_raw_buffer_load_b128(rs, off + 1024, 0, 16));
                f32x16 z = {0};
                f32x16 d = mfma32(pack8(u[0], u[1], u[2], u[3], v[0], v[1], v[2], v[3]), a ? pq1 : pq0, z);
                const float sc = g * __builtin_amdgcn_rcpf(d[0] + 1e-6f);
                bf16x8 o = pack8(d[8] * sc, d[9] * sc, d[10] * sc, d[11] * sc, d[12] * sc, d[13] * sc, d[14] * sc, d[15] * sc);
                __builtin_amdgcn_raw_buffer_store_b128(__builtin_bit_cast(v4u, o), rs,
                                                       W_OA + ((qt * 2 + rh) * 32 + hg * 4 + 2 * hp + a) * 1024 + l16, 0, 16);
            }
        }
        arrive_and_flag(cnt + 1, 4u, ws + W_F5 + (qt * 8 + hg) * 8, tag, l);
        STAMP(8);
        LAS char* wa = sm + hp * 32768 + (4 * (g4 >> 1) + q4) * 128 + (((2 * (g4 & 1) + (p4 >> 1)) ^ ((q4 & 2) << 1)) * 16) + (p4 & 1) * 8;
        LAS char* wb = sm + hp * 32768 + (4 * (g4 >> 1) + q4) * 128 + (((4 + 2 * (g4 & 1) + (p4 >> 1)) ^ ((q4 & 2) << 1)) * 16) + (p4 & 1) * 8;
        f32x16 c0 = {0}, c1 = {0};
        const int ob = ((qt * 2 + rh) * 32 + 16 * hp) * 1024 + l16;
        poll_flags(ws + W_F5 + (qt * 8 + 4 * hp) * 8, 4, tag, l);
        STAMP(9);
        {
            bf16x8 of[16];
#pragma unroll
            for (int i = 0; i < 16; ++i) of[i] = __builtin_bit_cast(bf16x8, __builtin_amdgcn_raw_buffer_load_b128(rs, W_OA + ob + i * 1024, 0, 16));
            poll_lds(cnt + 1, 4u);
#pragma unroll
            for (int i = 0; i < 16; ++i) {
                c0 = mfma32(ldtr(wa, L_WOA + i * 2048, L_WOA + i * 2048 + 1024), of[i], c0);
                c1 = mfma32(ldtr(wb, L_WOA + i * 2048, L_WOA + i * 2048 + 1024), of[i], c1);
            }
        }
        bf16x8 wf0[16], wf1[16];
        {
            LAS char* wa2 = wa + L_WOB; LAS char* wb2 = wb + L_WOB;
#pragma unroll
            for (int i = 0; i < 16; ++i) { wf0[i] = ldtr(wa2, i * 2048, i * 2048 + 1024); wf1[i] = ldtr(wb2, i * 2048, i * 2048 + 1024); }
#pragma unroll
            for (int i = 0; i < 16; ++i) asm volatile("" : "+v"(wf0[i]), "+v"(wf1[i]));
        }
        STAMP(10);
        poll_lds(cnt + 2, 4u);
        poll_flags(ws + W_F4 + (qt * 8 + 4 * hp) * 8, 4, tag, l);
        STAMP(11);
        {
            bf16x8 of[16];
#pragma unroll
            for (int i = 0; i < 16; ++i) of[i] = __builtin_bit_cast(bf16x8, __builtin_amdgcn_raw_buffer_load_b128(rs, W_OB + ob + i * 1024, 0, 16));
#pragma unroll
            for (int i = 0; i < 16; ++i) { c0 = mfma32(wf0[i], of[i], c0); c1 = mfma32(wf1[i], of[i], c1); }
        }
        f32x16 keep = hp ? c1 : c0, send = hp ? c0 : c1;
        LAS char* ex = sm + L_OS;
#pragma unroll
        for (int i = 0; i < 4; ++i)
            *(LAS f32x4*)(ex + w * 4096 + i * 1024 + l16) = (f32x4){send[4 * i], send[4 * i + 1], send[4 * i + 2], send[4 * i + 3]};
        wait_lgkm0();
        if (l == 0) __hip_atomic_store(cnt + 4 + w, 1u, RLX_WG);
        poll_lds(cnt + 4 + (w ^ 2), 1u);
        unsigned o2[8];
#pragma unroll
        for (int i = 0; i < 4; ++i) {
            f32x4 t = *(LAS f32x4*)(ex + (w ^ 2) * 4096 + i * 1024 + l16);
            o2[2 * i] = pk2(keep[4 * i] + t[0], keep[4 * i + 1] + t[1]);
            o2[2 * i + 1] = pk2(keep[4 * i + 2] + t[2], keep[4 * i + 3] + t[3]);
        }
        char* orow = p.out + (size_t)(64 * qt + 32 * rh + (l & 31)) * 1024 + hg * 128 + hp * 64 + (l >> 5) * 16;
#pragma unroll
        for (int k = 0; k < 2; ++k) {
            auto sx = __builtin_amdgcn_permlane32_swap(o2[4 * k], o2[4 * k + 2], false, false);
            auto sy = __builtin_amdgcn_permlane32_swap(o2[4 * k + 1], o2[4 * k + 3], false, false);
            *(GAS v4u*)(orow + k * 32) = (v4u){sx[0], sy[0], sx[1], sy[1]};
        }
        STAMP(12);
    } else {
        const int j = w - 4;
        __builtin_amdgcn_s_setprio(3);
        {
            const unsigned ring = sm0 + L_RING + j * RING_SZ;
            LAS char* rg = sm + L_RING + j * RING_SZ + (8 * (g4 >> 1) + q4) * 64 + (g4 & 1) * 32 + p4 * 8;
            const unsigned sa = (l >> 2) * 3072 + 1024 + ((l >> 1) & 1) * 1024 + hg * 128 + j * 32 + (l & 1) * 16;
            const unsigned sq = (l >> 2) * 3072 + hg * 128 + hp * 64 + (l & 3) * 16;
            LAS char* xa = sm + L_X + (l & 31) * XROW + (l >> 5) * 16;
#pragma unroll
            for (int s = 0; s < 7; ++s) { glds16(sa, p.wqkv + s * 49152, ring + s * 2048); glds16(sq, p.wqkv + s * 49152, ring + s * 2048 + 1024); }
            f32x16 t0 = {0}, t1 = {0}, tq = {0};
            bf16x8 fa, fq, x0, x1;
            wait_vm<12>();
            fa = ldtr(rg, 0, 256); fq = ldtr(rg, 1024, 1280); x0 = ld128(xa, 0); x1 = ld128(xa, 32 * XROW);
#pragma unroll
            for (int s = 0; s < 32; ++s) {
                bf16x8 na = fa, nq = fq, n0 = x0, n1 = x1;
                if (s + 1 < 32) {
                    if (s + 6 <= 31) wait_vm<10>();
                    else if (s == 26) wait_vm<8>(); else if (s == 27) wait_vm<6>(); else if (s == 28) wait_vm<4>();
                    else if (s == 29) wait_vm<2>(); else wait_vm<0>();
                    const int so = ((s + 1) % 7) * 2048;
                    na = ldtr(rg, so, so + 256); nq = ldtr(rg, so + 1024, so + 1280);
                    n0 = ld128(xa, (s + 1) * 32); n1 = ld128(xa, 32 * XROW + (s + 1) * 32);
                }
                SB();
                t0 = mfma32(x0, fa, t0); t1 = mfma32(x1, fa, t1); tq = mfma32(fq, rh ? x1 : x0, tq);
                SB();
                if (s + 7 < 32) {
                    glds16(sa, p.wqkv + (s + 7) * 49152, ring + (s % 7) * 2048);
                    glds16(sq, p.wqkv + (s + 7) * 49152, ring + (s % 7) * 2048 + 1024);
                }
                fa = na; fq = nq; x0 = n0; x1 = n1;
            }
            STAMP(5);
#pragma unroll
            for (int r = 0; r < 16; ++r) tq[r] = phi(tq[r]);
            *(LAS bf16x8*)(sm + L_PQ + j * 2048 + l16) = pack8(tq[0], tq[1], tq[2], tq[3], tq[4], tq[5], tq[6], tq[7]);
            *(LAS bf16x8*)(sm + L_PQ + j * 2048 + 1024 + l16) = pack8(tq[8], tq[9], tq[10], tq[11], tq[12], tq[13], tq[14], tq[15]);
            const bool kl = (l & 16) == 0;
            f32x16 R = {0};
#pragma unroll
            for (int h2 = 0; h2 < 4; ++h2) {
                const f32x16& T = h2 < 2 ? t0 : t1;
                const int o = (h2 & 1) * 8;
                bf16x8 af = pack8(phi(T[o]), phi(T[o + 1]), phi(T[o + 2]), phi(T[o + 3]), phi(T[o + 4]), phi(T[o + 5]), phi(T[o + 6]), phi(T[o + 7]));
                v4u bv = __builtin_bit_cast(v4u, pack8(T[o], T[o + 1], T[o + 2], T[o + 3], T[o + 4], T[o + 5], T[o + 6], T[o + 7]));
#pragma unroll
                for (int i = 0; i < 4; ++i) bv[i] = kl ? 0x3f803f80u : bv[i];
                R = mfma32(af, __builtin_bit_cast(bf16x8, bv), R);
            }
            const int off = W_KVP + (hg * 32 + qt) * 8192 + j * 2048 + l16;
            __builtin_amdgcn_raw_buffer_store_b128(__builtin_bit_cast(v4u, (f32x4){R[0], R[1], R[2], R[3]}), rs, off, 0, 16);
            __builtin_amdgcn_raw_buffer_store_b128(__builtin_bit_cast(v4u, (f32x4){R[4], R[5], R[6], R[7]}), rs, off + 1024, 0, 16);
            arrive_and_flag(cnt + 0, 4u, ws + W_F2 + (hg * 32 + qt) * 8, tag, l);
            STAMP(6);
        }
        bf16x8 Kf[8], Vf[16];
        poll_flags(ws + W_F1 + w * 256, 32, tag, l);
        load_kv(Kf, Vf, rs, w, l);
        if (j == 0) {
            poll_flags(ws + W_F2 + hg * 256, 32, tag, l);
            float v[32];
#pragma unroll
            for (int i = 0; i < 32; ++i)
                v[i] = __uint_as_float(__builtin_amdgcn_raw_buffer_load_b32(rs, W_KVP + (hg * 32 + i) * 8192 + qt * 256 + l * 4, 0, 16));
            float s = 0.f;
#pragma unroll
            for (int i = 0; i < 32; ++i) s += v[i];
            __builtin_amdgcn_raw_buffer_store_b32(__float_as_uint(s), rs, W_KVS + hg * 8192 + qt * 256 + l * 4, 0, 16);
            wait_vm<0>();
            if (l == 0) __hip_atomic_store((gu64*)(ws + W_F3) + hg * 32 + qt, tag, RLX_AGENT);
        }
        STAMP(7);
        __builtin_amdgcn_s_setprio(0);
        f32x16 O0, O1;
        attention(Kf, Vf, sm, j, 4u, l, O0, O1);
        STAMP(8);
        const int c = (2 * j + 6) & 7;
        if (__builtin_expect(__builtin_amdgcn_ballot_w64(unsafe_sum(O0)) != 0, 0)) slow_tile(O0, ld128(sm, L_Q + c * 1024 + l16), rs, l);
        if (__builtin_expect(__builtin_amdgcn_ballot_w64(unsafe_sum(O1)) != 0, 0)) slow_tile(O1, ld128(sm, L_Q + (c + 1) * 1024 + l16), rs, l);
        const float g1 = 1.f - *(LAS float*)(sm + L_G + (32 * (c >> 2) + (l & 31)) * 4);
        const float sa = g1 * __builtin_amdgcn_rcpf(O0[8]), sb = g1 * __builtin_amdgcn_rcpf(O1[8]);
        const int off = W_OB + ((qt * 2 + (c >> 2)) * 32 + hg * 4 + (c & 3)) * 1024 + l16;
        bf16x8 oa = pack8(O0[0] * sa, O0[1] * sa, O0[2] * sa, O0[3] * sa, O0[4] * sa, O0[5] * sa, O0[6] * sa, O0[7] * sa);
        bf16x8 ob = pack8(O1[0] * sb, O1[1] * sb, O1[2] * sb, O1[3] * sb, O1[4] * sb, O1[5] * sb, O1[6] * sb, O1[7] * sb);
        __builtin_amdgcn_raw_buffer_store_b128(__builtin_bit_cast(v4u, oa), rs, off, 0, 16);
        __builtin_amdgcn_raw_buffer_store_b128(__builtin_bit_cast(v4u, ob), rs, off + 1024, 0, 16);
        arrive_and_flag(cnt + 2, 4u, ws + W_F4 + (qt * 8 + hg) * 8, tag, l);
        if (b == 0 && j == 0 && l == 0) __hip_atomic_store((gu64*)(ws + W_E), tag, RLX_AGENT);
        STAMP(9);
    }
}

extern "C" void kernel_launch(void* const* d_in, const int* in_sizes, int n_in, void* d_out, int out_size, void* d_ws,
                              size_t ws_size, hipStream_t stream) {
    Params p;
    p.x = (const char*)d_in[0]; p.wqkv = (const char*)d_in[1]; p.woa = (const char*)d_in[2];
    p.wq = (const char*)d_in[4]; p.wk = (const char*)d_in[5]; p.wv = (const char*)d_in[6];
    p.wob = (const char*)d_in[7]; p.gw = (const char*)d_in[8];
    p.out = (char*)d_out; p.ws = (char*)d_ws;
    fused_kernel<<<256, 512, 0, stream>>>(p);
}
```

```cpp
#include <hip/hip_runtime.h>
#include <hip/hip_bf16.h>
#include <stdint.h>

#define LAS __attribute__((address_space(3)))
#define GAS __attribute__((address_space(1)))
typedef __attribute__((ext_vector_type(8))) __bf16 bf16x8;
typedef __attribute__((ext_vector_type(4))) __bf16 bf16x4;
typedef __attribute__((ext_vector_type(2))) __bf16 bf16x2;
typedef __attribute__((ext_vector_type(16))) __bf16 bf16x16;
typedef __attribute__((ext_vector_type(16))) float f32x16;
typedef __attribute__((ext_vector_type(8))) float f32x8;
typedef __attribute__((ext_vector_type(4))) float f32x4;
typedef __attribute__((ext_vector_type(2))) float f32x2;
typedef unsigned v4u __attribute__((ext_vector_type(4)));
typedef unsigned v2u __attribute__((ext_vector_type(2)));
typedef unsigned long long u64;
typedef GAS u64 gu64;

#define SETPRIO(x) __builtin_amdgcn_s_setprio(x)
#define V_SL1 4
#define V_SL2 2
#define SB() __builtin_amdgcn_sched_barrier(0)
#define PIN(x) do { asm volatile("" : "+v"(x)); SB(); } while (0)
#define CFENCE() asm volatile("" ::: "memory")
#define RLX_AGENT __ATOMIC_RELAXED, __HIP_MEMORY_SCOPE_AGENT
#define RLX_WG __ATOMIC_RELAXED, __HIP_MEMORY_SCOPE_WORKGROUP

constexpr int XROW = 1040;
constexpr int L_X = 0;
constexpr int L_WQ = 66560;
constexpr int L_RING = 66560, RING_SZ = 14336;
constexpr int L_OS1 = 111616;
constexpr int L_WKV = 132096;
constexpr int L_Q = 132096;
constexpr int L_OS0 = 140288;
constexpr int PAIR = 5120, P_TAG = 4096;
constexpr int L_GW = 160768;
constexpr int L_G = 161792;
constexpr int L_CNT = 162048;
constexpr int L_GP = 162304;
constexpr int L_TAG = 163328;
constexpr int L_DUMP = 163584;
constexpr int L_TOTAL = 163840;

constexpr int W_E = 0;
constexpr int W_F1 = 4096;
constexpr int W_F6 = 262144;
constexpr int W_K = 65536;
constexpr int W_V = 131072;
constexpr int W_KVS = 196608;
constexpr int W_KVP = 1 << 20;
constexpr int W_OA = 4 << 20;
constexpr int W_OB = 6 << 20;
constexpr int W_END = 8 << 20;
constexpr int W_DBG = 16 << 20;

constexpr int NPF = 4;
#define V_MAPQ 4
constexpr int MAPQ = V_MAPQ, NHG = 32 / MAPQ;
constexpr unsigned SENT = 0xFFFFFFFFu;
constexpr unsigned CODE_LINES = 302, CODE_BACK = 128;
constexpr unsigned SPIN_CAP = 1u << 18;

struct Params {
    const char* x; const char* wqkv; const char* woa; const char* wq; const char* wk; const char* wv;
    const char* wob; const char* gw; char* out; char* ws;
};

struct Stamps {};
#define STAMP(i) do {} while (0)
#define STAMPD(i) do {} while (0)
#define STAMP_FLUSH() do {} while (0)

__device__ __forceinline__ void glds16(unsigned voff, const char* base, unsigned lds) {
    asm volatile("s_mov_b32 m0, %2\n\ts_nop 0\n\tglobal_load_lds_dwordx4 %0, %1" :: "v"(voff), "s"(base), "s"(lds) : "memory");
}
__device__ __forceinline__ v4u make_rs(const char* p, unsigned bytes) {
    return (v4u){(unsigned)(uintptr_t)p, (unsigned)((uintptr_t)p >> 32), bytes, 0x00020000u};
}
template <int N, int SS, int LS>
__device__ __forceinline__ void glds_run(unsigned voff, v4u rs, unsigned so, unsigned lds) {
    unsigned t;
    asm volatile("s_mov_b32 m0, %4\n\ts_mov_b32 %0, %3\n\tbuffer_load_dwordx4 %1, %2, %0 offen lds\n\t"
                 ".rept %5\n\ts_add_u32 m0, m0, %7\n\ts_add_u32 %0, %0, %6\n\tbuffer_load_dwordx4 %1, %2, %0 offen lds\n\t.endr"
                 : "=&s"(t) : "v"(voff), "s"(rs), "s"(so), "s"(lds), "n"(N - 1), "n"(SS), "n"(LS) : "memory", "scc");
}
template <int SO, int SS, int LO>
__device__ __forceinline__ void glds_pair(unsigned voff, v4u rs, unsigned so, unsigned lds) {
    unsigned t;
    asm volatile("s_add_u32 m0, %4, %7\n\ts_add_u32 %0, %3, %5\n\tbuffer_load_dwordx4 %1, %2, %0 offen lds\n\t"
                 "s_add_u32 m0, m0, 1024\n\ts_add_u32 %0, %0, %6\n\tbuffer_load_dwordx4 %1, %2, %0 offen lds"
                 : "=&s"(t) : "v"(voff), "s"(rs), "s"(so), "s"(lds), "n"(SO), "n"(SS), "n"(LO) : "memory", "scc");
}
__device__ __forceinline__ void glds4(unsigned voff, const char* base, unsigned lds) {
    asm volatile("s_mov_b32 m0, %2\n\ts_nop 0\n\tglobal_load_lds_dword %0, %1" :: "v"(voff), "s"(base), "s"(lds) : "memory");
}
__device__ __forceinline__ void sent1(v4u v, v4u rsv, unsigned off, unsigned soff) {
    asm volatile("buffer_store_dwordx4 %0, %1, %2, %3 offen sc1" :: "v"(v), "v"(off), "s"(rsv), "s"(soff) : "memory");
}
template <int N> __device__ __forceinline__ void wait_vm() { asm volatile("s_waitcnt vmcnt(%0)" :: "n"(N) : "memory"); }
__device__ __forceinline__ void wait_lgkm0() { asm volatile("s_waitcnt lgkmcnt(0)" ::: "memory"); }
__device__ __forceinline__ void bar() { asm volatile("s_waitcnt lgkmcnt(0)\n\ts_barrier" ::: "memory"); }

__device__ __forceinline__ unsigned pk2(float a, float b) { bf16x2 t = {(__bf16)a, (__bf16)b}; return __builtin_bit_cast(unsigned, t); }
__device__ __forceinline__ bf16x8 pack8(float a0, float a1, float a2, float a3, float a4, float a5, float a6, float a7) {
    v4u v = {pk2(a0, a1), pk2(a2, a3), pk2(a4, a5), pk2(a6, a7)};
    return __builtin_bit_cast(bf16x8, v);
}
__device__ __forceinline__ bf16x8 ld128(LAS char* s, int off) { return *(LAS bf16x8*)(s + off); }
__device__ __forceinline__ bf16x8 ldtr(LAS char* s, int off0, int off1) {
    bf16x4 a = __builtin_amdgcn_ds_read_tr16_b64_v4bf16((LAS bf16x4*)(s + off0));
    bf16x4 b = __builtin_amdgcn_ds_read_tr16_b64_v4bf16((LAS bf16x4*)(s + off1));
    return __builtin_shufflevector(a, b, 0, 1, 2, 3, 4, 5, 6, 7);
}
__device__ __forceinline__ f32x16 mfma32(const bf16x8& a, const bf16x8& b, const f32x16& c) {
    return __builtin_amdgcn_mfma_f32_32x32x16_bf16(a, b, c, 0, 0, 0);
}
__device__ __forceinline__ bf16x16 lo8(const f32x16& s) { f32x8 t = __builtin_shufflevector(s, s, 0, 1, 2, 3, 4, 5, 6, 7); return __builtin_bit_cast(bf16x16, t); }
__device__ __forceinline__ bf16x16 hi8(const f32x16& s) { f32x8 t = __builtin_shufflevector(s, s, 8, 9, 10, 11, 12, 13, 14, 15); return __builtin_bit_cast(bf16x16, t); }
__device__ __forceinline__ f32x16 pv(const bf16x8& v, const bf16x16& pr, const f32x16& o, int idx) {
    return __builtin_amdgcn_smfmac_f32_32x32x32_bf16(v, pr, o, idx, 0, 0);
}
constexpr float PHI_EPS = 1e-6f;
__device__ __forceinline__ unsigned phi2(float a, float b) {
    const f32x2 u = (f32x2){a, b} * 1.44269504088896341f;
    const f32x2 e = {__builtin_amdgcn_fmed3f(__builtin_amdgcn_exp2f(u[0]), 0.f, 1.f), __builtin_amdgcn_fmed3f(__builtin_amdgcn_exp2f(u[1]), 0.f, 1.f)};
    float ma, mb;
    asm("v_max_f32 %0, %1, %2" : "=v"(ma) : "s"(PHI_EPS), "v"(a));
    asm("v_max_f32 %0, %1, %2" : "=v"(mb) : "s"(PHI_EPS), "v"(b));
    const f32x2 r = (f32x2){ma, mb} + e;
    return pk2(r[0], r[1]);
}

__device__ __forceinline__ void poll_flags(const char* base, u64 tag, int l) {
    const gu64* f = (const gu64*)base + (l & 31);
#pragma unroll 1
    for (unsigned s = 0; s < SPIN_CAP; ++s) {
        const u64 v = __hip_atomic_load(f, RLX_AGENT);
        if (__builtin_amdgcn_ballot_w64(v != tag) == 0) break;
        __builtin_amdgcn_s_sleep(4);
    }
    CFENCE();
}
template <int SLEEP = 0>
__device__ __forceinline__ void poll_lds_ge(LAS unsigned* c, unsigned want) {
    unsigned v, t, n;
    asm volatile("s_mov_b32 %2, 0x400000\n"
                 ".Lp_%=:\n\t"
                 "ds_read_b32 %0, %3\n\t"
                 "s_waitcnt lgkmcnt(0)\n\t"
                 "v_readfirstlane_b32 %1, %0\n\t"
                 "s_cmp_ge_u32 %1, %4\n\t"
                 "s_cbranch_scc1 .Lq_%=\n\t"
                 "s_sleep %5\n\t"
                 "s_sub_u32 %2, %2, 1\n\t"
                 "s_cbranch_scc0 .Lp_%=\n"
                 ".Lq_%=:"
                 : "=&v"(v), "=&s"(t), "=&s"(n) : "v"(c), "s"(want), "n"(SLEEP) : "memory", "scc");
}
#define V_C1DELAY 24
#define V_ROT (rk >> 2)
#define V_TW 8u
#define V_TQ 8
#define V_PD 0
#define V_TP 14
#define V_SP 4
#define V_T0 3
#define V_T1 12
#define V_BGW 8u
#define V_PACE 1
#define PACE() asm volatile("s_nop 7")
#define V_GAP 5
#define GAP() do { SB(); asm volatile("s_nop %0" :: "n"(V_GAP)); SB(); } while (0)
__device__ __forceinline__ void lds_arrive(unsigned addr) {
    asm volatile("s_mov_b64 exec, 1\n\tds_add_u32 %0, %1\n\ts_mov_b64 exec, -1" :: "v"(addr), "v"(1u) : "memory");
}
#define EX(S, i) S[i] = __builtin_amdgcn_exp2f(S[i])
struct KV { f32x16 k0, k1, v0, v1, v2, v3; };

#define TOFF(i) [t##i] "s"(((rot + i) & 7) * 1024)
__device__ __forceinline__ void load_kv_first(KV& r, f32x16& O0, f32x16& O1, v4u rsv, unsigned ko, unsigned vo, unsigned one, int rot, unsigned kcnt) {
    asm volatile(
        "s_nop 4\n\t"
        "buffer_load_dwordx4 v[68:71], %[ko], %[rs], %[t0] offen sc1\n\t"
        "buffer_load_dwordx4 v[72:75], %[ko], %[rs], %[t1] offen sc1\n\t"
        "buffer_load_dwordx4 v[100:103], %[vo], %[rs], %[t0] offen sc1\n\t"
        "buffer_load_dwordx4 v[76:79], %[ko], %[rs], %[t2] offen sc1\n\t"
        "buffer_load_dwordx4 v[108:111], %[vo], %[rs], %[t1] offen sc1\n\t"
        "buffer_load_dwordx4 v[80:83], %[ko], %[rs], %[t3] offen sc1\n\t"
        "buffer_load_dwordx4 v[116:119], %[vo], %[rs], %[t2] offen sc1\n\t"
        "buffer_load_dwordx4 v[84:87], %[ko], %[rs], %[t4] offen sc1\n\t"
        "buffer_load_dwordx4 v[124:127], %[vo], %[rs], %[t3] offen sc1\n\t"
        "buffer_load_dwordx4 v[88:91], %[ko], %[rs], %[t5] offen sc1\n\t"
        "buffer_load_dwordx4 v[132:135], %[vo], %[rs], %[t4] offen sc1\n\t"
        "buffer_load_dwordx4 v[92:95], %[ko], %[rs], %[t6] offen sc1\n\t"
        "buffer_load_dwordx4 v[140:143], %[vo], %[rs], %[t5] offen sc1\n\t"
        "buffer_load_dwordx4 v[96:99], %[ko], %[rs], %[t7] offen sc1\n\t"
        "buffer_load_dwordx4 v[148:151], %[vo], %[rs], %[t6] offen sc1\n\t"
        "buffer_load_dwordx4 v[156:159], %[vo], %[rs], %[t7] offen sc1\n\t"
        "s_mov_b64 exec, 1\n\t"
        "ds_add_u32 %[kc], %[k1]\n\t"
        "s_mov_b64 exec, -1\n\t"
        "v_mov_b32 v104, %[one]\n\t"
        "v_mov_b32 v105, %[one]\n\t"
        "v_mov_b32 v106, %[one]\n\t"
        "v_mov_b32 v107, %[one]\n\t"
        "v_mov_b32 v112, %[one]\n\t"
        "v_mov_b32 v113, %[one]\n\t"
        "v_mov_b32 v114, %[one]\n\t"
        "v_mov_b32 v115, %[one]\n\t"
        "v_mov_b32 v120, %[one]\n\t"
        "v_mov_b32 v121, %[one]\n\t"
        "v_mov_b32 v122, %[one]\n\t"
        "v_mov_b32 v123, %[one]\n\t"
        "v_mov_b32 v128, %[one]\n\t"
        "v_mov_b32 v129, %[one]\n\t"
        "v_mov_b32 v130, %[one]\n\t"
        "v_mov_b32 v131, %[one]\n\t"
        "v_mov_b32 v136, %[one]\n\t"
        "v_mov_b32 v137, %[one]\n\t"
        "v_mov_b32 v138, %[one]\n\t"
        "v_mov_b32 v139, %[one]\n\t"
        "v_mov_b32 v144, %[one]\n\t"
        "v_mov_b32 v145, %[one]\n\t"
        "v_mov_b32 v146, %[one]\n\t"
        "v_mov_b32 v147, %[one]\n\t"
        "v_mov_b32 v152, %[one]\n\t"
        "v_mov_b32 v153, %[one]\n\t"
        "v_mov_b32 v154, %[one]\n\t"
        "v_mov_b32 v155, %[one]\n\t"
        "v_mov_b32 v160, %[one]\n\t"
        "v_mov_b32 v161, %[one]\n\t"
        "v_mov_b32 v162, %[one]\n\t"
        "v_mov_b32 v163, %[one]\n\t"
        "v_mov_b64 v[0:1], 0\n\t"
        "v_mov_b64 v[2:3], 0\n\t"
        "v_mov_b64 v[4:5], 0\n\t"
        "v_mov_b64 v[6:7], 0\n\t"
        "v_mov_b64 v[8:9], 0\n\t"
        "v_mov_b64 v[10:11], 0\n\t"
        "v_mov_b64 v[12:13], 0\n\t"
        "v_mov_b64 v[14:15], 0\n\t"
        "v_mov_b64 v[16:17], 0\n\t"
        "v_mov_b64 v[18:19], 0\n\t"
        "v_mov_b64 v[20:21], 0\n\t"
        "v_mov_b64 v[22:23], 0\n\t"
        "v_mov_b64 v[24:25], 0\n\t"
        : "=&{v[0:15]}"(O0), "=&{v[16:31]}"(O1), "=&{v[68:83]}"(r.k0), "=&{v[84:99]}"(r.k1), "=&{v[100:115]}"(r.v0), "=&{v[116:131]}"(r.v1), "=&{v[132:147]}"(r.v2), "=&{v[148:163]}"(r.v3)
        : [ko] "v"(ko), [vo] "v"(vo), [one] "v"(one), [kc] "v"(kcnt), [k1] "v"(1u), [rs] "s"(rsv), TOFF(0), TOFF(1), TOFF(2), TOFF(3), TOFF(4), TOFF(5), TOFF(6), TOFF(7)
        : "memory");
}

__device__ __forceinline__ void attention(KV& r, f32x16& O0, f32x16& O1, unsigned l16, unsigned os, unsigned ox, unsigned qs, int pos,
                                          unsigned vb, unsigned second, v4u rsv, int rot, unsigned kcnt, unsigned c9, unsigned sg, Stamps& st) {
    asm volatile(
        "v_add_u32 v29, %[sq0], %[l16]\n\t"
        "ds_read_b128 v[64:67], v29\n\t"
        "s_mov_b32 s40, %[vb]\n\t"
        "s_add_u32 s41, %[vb], 1\n\t"
        "s_add_u32 s42, %[vb], 2\n\t"
        "s_add_u32 s43, %[vb], 3\n\t"
        "s_add_u32 s44, %[vb], 4\n\t"
        "s_mov_b32 s45, 0x100000\n\t"
        "s_cmp_lg_u32 %[second], 0\n\t"
        "s_cselect_b64 s[46:47], -1, 0\n\t"
        "s_setprio 2\n\t"
        "s_waitcnt vmcnt(13) lgkmcnt(0)\n\t"
        "v_mfma_f32_32x32x16_bf16 v[32:47], v[68:71], v[64:67], 0\n\t"
        "v_permlane16_swap_b32 v100, v104\n\t"
        "v_permlane16_swap_b32 v101, v105\n\t"
        "v_permlane16_swap_b32 v102, v106\n\t"
        "v_permlane16_swap_b32 v103, v107\n\t"
        "s_nop 7\n\t"
        "v_exp_f32 v32, v32\n\t"
        "v_exp_f32 v33, v33\n\t"
        "v_exp_f32 v34, v34\n\t"
        "v_exp_f32 v35, v35\n\t"
        "v_exp_f32 v36, v36\n\t"
        "v_mfma_f32_32x32x16_bf16 v[48:63], v[72:75], v[64:67], 0\n\t"
        "v_exp_f32 v37, v37\n\t"
        "v_exp_f32 v38, v38\n\t"
        "v_exp_f32 v39, v39\n\t"
        "v_exp_f32 v40, v40\n\t"
        "v_exp_f32 v41, v41\n\t"
        "v_smfmac_f32_32x32x32_bf16 v[0:15], v[100:103], v[32:39], %[idx]\n\t"
        "v_exp_f32 v42, v42\n\t"
        "v_exp_f32 v43, v43\n\t"
        "v_exp_f32 v44, v44\n\t"
        "v_exp_f32 v45, v45\n\t"
        "v_exp_f32 v46, v46\n\t"
        "v_exp_f32 v47, v47\n\t"
        "v_exp_f32 v48, v48\n\t"
        "v_exp_f32 v49, v49\n\t"
        "v_smfmac_f32_32x32x32_bf16 v[0:15], v[104:107], v[40:47], %[idx]\n\t"
        "v_exp_f32 v50, v50\n\t"
        "s_waitcnt vmcnt(11)\n\t"
        "v_exp_f32 v51, v51\n\t"
        "v_exp_f32 v52, v52\n\t"
        "v_mfma_f32_32x32x16_bf16 v[32:47], v[76:79], v[64:67], 0\n\t"
        "v_exp_f32 v53, v53\n\t"
        "v_permlane16_swap_b32 v108, v112\n\t"
        "v_permlane16_swap_b32 v109, v113\n\t"
        "v_permlane16_swap_b32 v110, v114\n\t"
        "v_permlane16_swap_b32 v111, v115\n\t"
        "v_exp_f32 v54, v54\n\t"
        "v_exp_f32 v55, v55\n\t"
        "v_exp_f32 v56, v56\n\t"
        "v_exp_f32 v57, v57\n\t"
        "v_smfmac_f32_32x32x32_bf16 v[0:15], v[108:111], v[48:55], %[idx]\n\t"
        "v_exp_f32 v58, v58\n\t"
        "v_exp_f32 v59, v59\n\t"
        "v_exp_f32 v60, v60\n\t"
        "v_exp_f32 v61, v61\n\t"
        "v_exp_f32 v62, v62\n\t"
        "v_exp_f32 v63, v63\n\t"
        "v_exp_f32 v32, v32\n\t"
        "v_exp_f32 v33, v33\n\t"
        "v_smfmac_f32_32x32x32_bf16 v[0:15], v[112:115], v[56:63], %[idx]\n\t"
        "v_exp_f32 v34, v34\n\t"
        "s_waitcnt vmcnt(9)\n\t"
        "v_exp_f32 v35, v35\n\t"
        "v_exp_f32 v36, v36\n\t"
        "v_mfma_f32_32x32x16_bf16 v[48:63], v[80:83], v[64:67], 0\n\t"
        "v_exp_f32 v37, v37\n\t"
        "v_permlane16_swap_b32 v116, v120\n\t"
        "v_permlane16_swap_b32 v117, v121\n\t"
        "v_permlane16_swap_b32 v118, v122\n\t"
        "v_permlane16_swap_b32 v119, v123\n\t"
        "v_exp_f32 v38, v38\n\t"
        "v_exp_f32 v39, v39\n\t"
        "v_exp_f32 v40, v40\n\t"
        "v_exp_f32 v41, v41\n\t"
        "v_smfmac_f32_32x32x32_bf16 v[0:15], v[116:119], v[32:39], %[idx]\n\t"
        "v_exp_f32 v42, v42\n\t"
        "v_exp_f32 v43, v43\n\t"
        "v_exp_f32 v44, v44\n\t"
        "v_exp_f32 v45, v45\n\t"
        "v_exp_f32 v46, v46\n\t"
        "v_exp_f32 v47, v47\n\t"
        "v_exp_f32 v48, v48\n\t"
        "v_exp_f32 v49, v49\n\t"
        "v_smfmac_f32_32x32x32_bf16 v[0:15], v[120:123], v[40:47], %[idx]\n\t"
        "v_exp_f32 v50, v50\n\t"
        "s_waitcnt vmcnt(7)\n\t"
        "v_exp_f32 v51, v51\n\t"
        "v_exp_f32 v52, v52\n\t"
        "v_mfma_f32_32x32x16_bf16 v[32:47], v[84:87], v[64:67], 0\n\t"
        "v_exp_f32 v53, v53\n\t"
        "v_permlane16_swap_b32 v124, v128\n\t"
        "v_permlane16_swap_b32 v125, v129\n\t"
        "v_permlane16_swap_b32 v126, v130\n\t"
        "v_permlane16_swap_b32 v127, v131\n\t"
        "v_exp_f32 v54, v54\n\t"
        "v_exp_f32 v55, v55\n\t"
        "v_exp_f32 v56, v56\n\t"
        "v_exp_f32 v57, v57\n\t"
        "v_smfmac_f32_32x32x32_bf16 v[0:15], v[124:127], v[48:55], %[idx]\n\t"
        "v_exp_f32 v58, v58\n\t"
        "v_exp_f32 v59, v59\n\t"
        "v_exp_f32 v60, v60\n\t"
        "v_exp_f32 v61, v61\n\t"
        "v_exp_f32 v62, v62\n\t"
        "v_exp_f32 v63, v63\n\t"
        "v_exp_f32 v32, v32\n\t"
        "v_exp_f32 v33, v33\n\t"
        "v_smfmac_f32_32x32x32_bf16 v[0:15], v[128:131], v[56:63], %[idx]\n\t"
        "v_exp_f32 v34, v34\n\t"
        "s_waitcnt vmcnt(5)\n\t"
        "v_exp_f32 v35, v35\n\t"
        "v_exp_f32 v36, v36\n\t"
        "v_mfma_f32_32x32x16_bf16 v[48:63], v[88:91], v[64:67], 0\n\t"
        "v_exp_f32 v37, v37\n\t"
        "v_permlane16_swap_b32 v132, v136\n\t"
        "v_permlane16_swap_b32 v133, v137\n\t"
        "v_permlane16_swap_b32 v134, v138\n\t"
        "v_permlane16_swap_b32 v135, v139\n\t"
        "v_exp_f32 v38, v38\n\t"
        "v_exp_f32 v39, v39\n\t"
        "v_exp_f32 v40, v40\n\t"
        "v_exp_f32 v41, v41\n\t"
        "v_smfmac_f32_32x32x32_bf16 v[0:15], v[132:135], v[32:39], %[idx]\n\t"
        "v_exp_f32 v42, v42\n\t"
        "v_exp_f32 v43, v43\n\t"
        "v_exp_f32 v44, v44\n\t"
        "v_exp_f32 v45, v45\n\t"
        "v_exp_f32 v46, v46\n\t"
        "v_exp_f32 v47, v47\n\t"
        "v_exp_f32 v48, v48\n\t"
        "v_exp_f32 v49, v49\n\t"
        "v_smfmac_f32_32x32x32_bf16 v[0:15], v[136:139], v[40:47], %[idx]\n\t"
        "v_exp_f32 v50, v50\n\t"
        "s_waitcnt vmcnt(3)\n\t"
        "v_exp_f32 v51, v51\n\t"
        "v_exp_f32 v52, v52\n\t"
        "v_mfma_f32_32x32x16_bf16 v[32:47], v[92:95], v[64:67], 0\n\t"
        "v_exp_f32 v53, v53\n\t"
        "v_permlane16_swap_b32 v140, v144\n\t"
        "v_permlane16_swap_b32 v141, v145\n\t"
        "v_permlane16_swap_b32 v142, v146\n\t"
        "v_permlane16_swap_b32 v143, v147\n\t"
        "v_exp_f32 v54, v54\n\t"
        "v_exp_f32 v55, v55\n\t"
        "v_exp_f32 v56, v56\n\t"
        "v_exp_f32 v57, v57\n\t"
        "v_smfmac_f32_32x32x32_bf16 v[0:15], v[140:143], v[48:55], %[idx]\n\t"
        "v_exp_f32 v58, v58\n\t"
        "v_exp_f32 v59, v59\n\t"
        "v_exp_f32 v60, v60\n\t"
        "v_add_u32 v29, %[sq0], %[l16]\n\t"
        "v_exp_f32 v61, v61\n\t"
        "v_exp_f32 v62, v62\n\t"
        "v_exp_f32 v63, v63\n\t"
        "v_exp_f32 v32, v32\n\t"
        "v_exp_f32 v33, v33\n\t"
        "v_smfmac_f32_32x32x32_bf16 v[0:15], v[144:147], v[56:63], %[idx]\n\t"
        "v_exp_f32 v34, v34\n\t"
        "s_waitcnt vmcnt(1)\n\t"
        "v_exp_f32 v35, v35\n\t"
        "v_exp_f32 v36, v36\n\t"
        "v_mfma_f32_32x32x16_bf16 v[48:63], v[96:99], v[64:67], 0\n\t"
        "v_exp_f32 v37, v37\n\t"
        "v_permlane16_swap_b32 v148, v152\n\t"
        "v_permlane16_swap_b32 v149, v153\n\t"
        "v_permlane16_swap_b32 v150, v154\n\t"
        "v_permlane16_swap_b32 v151, v155\n\t"
        "v_exp_f32 v38, v38\n\t"
        "ds_read_b128 v[64:67], v29 offset:1024\n\t"
        "v_exp_f32 v39, v39\n\t"
        "v_exp_f32 v40, v40\n\t"
        "v_exp_f32 v41, v41\n\t"
        "v_smfmac_f32_32x32x32_bf16 v[0:15], v[148:151], v[32:39], %[idx]\n\t"
        "v_exp_f32 v42, v42\n\t"
        "v_exp_f32 v43, v43\n\t"
        "v_exp_f32 v44, v44\n\t"
        "v_exp_f32 v45, v45\n\t"
        "v_exp_f32 v46, v46\n\t"
        "v_exp_f32 v47, v47\n\t"
        "v_exp_f32 v48, v48\n\t"
        "v_exp_f32 v49, v49\n\t"
        "v_smfmac_f32_32x32x32_bf16 v[0:15], v[152:155], v[40:47], %[idx]\n\t"
        "v_exp_f32 v50, v50\n\t"
        "s_waitcnt vmcnt(0)\n\t"
        "s_waitcnt lgkmcnt(0)\n\t"
        "v_exp_f32 v51, v51\n\t"
        "v_exp_f32 v52, v52\n\t"
        "v_mfma_f32_32x32x16_bf16 v[32:47], v[68:71], v[64:67], 0\n\t"
        "v_exp_f32 v53, v53\n\t"
        "v_permlane16_swap_b32 v156, v160\n\t"
        "v_permlane16_swap_b32 v157, v161\n\t"
        "v_permlane16_swap_b32 v158, v162\n\t"
        "v_permlane16_swap_b32 v159, v163\n\t"
        "v_exp_f32 v54, v54\n\t"
        "v_exp_f32 v55, v55\n\t"
        "v_exp_f32 v56, v56\n\t"
        "v_exp_f32 v57, v57\n\t"
        "v_smfmac_f32_32x32x32_bf16 v[0:15], v[156:159], v[48:55], %[idx]\n\t"
        "v_exp_f32 v58, v58\n\t"
        "v_exp_f32 v59, v59\n\t"
        "v_exp_f32 v60, v60\n\t"
        "v_exp_f32 v61, v61\n\t"
        "v_exp_f32 v62, v62\n\t"
        "v_exp_f32 v63, v63\n\t"
        "v_exp_f32 v32, v32\n\t"
        "v_exp_f32 v33, v33\n\t"
        "v_smfmac_f32_32x32x32_bf16 v[0:15], v[160:163], v[56:63], %[idx]\n\t"
        "v_exp_f32 v34, v34\n\t"
        "v_exp_f32 v35, v35\n\t"
        "v_exp_f32 v36, v36\n\t"
        "v_mfma_f32_32x32x16_bf16 v[48:63], v[72:75], v[64:67], 0\n\t"
        "v_exp_f32 v37, v37\n\t"
        "v_exp_f32 v38, v38\n\t"
        "v_exp_f32 v39, v39\n\t"
        "v_exp_f32 v40, v40\n\t"
        "v_exp_f32 v41, v41\n\t"
        "v_smfmac_f32_32x32x32_bf16 v[16:31], v[100:103], v[32:39], %[idx]\n\t"
        "v_exp_f32 v42, v42\n\t"
        "v_exp_f32 v43, v43\n\t"
        "v_exp_f32 v44, v44\n\t"
        "v_exp_f32 v45, v45\n\t"
        "v_exp_f32 v46, v46\n\t"
        "v_exp_f32 v47, v47\n\t"
        "v_exp_f32 v48, v48\n\t"
        "v_add_u32 v12, %[so0], %[l16]\n\t"
        "v_mov_b32 v9, s41\n\t"
        "v_exp_f32 v49, v49\n\t"
        "v_smfmac_f32_32x32x32_bf16 v[16:31], v[104:107], v[40:47], %[idx]\n\t"
        "v_exp_f32 v50, v50\n\t"
        "ds_write_b128 v12, v[0:3] offset:0\n\t"
        "v_exp_f32 v51, v51\n\t"
        "v_exp_f32 v52, v52\n\t"
        "v_mfma_f32_32x32x16_bf16 v[32:47], v[76:79], v[64:67], 0\n\t"
        "v_exp_f32 v53, v53\n\t"
        "v_exp_f32 v54, v54\n\t"
        "v_exp_f32 v55, v55\n\t"
        "v_exp_f32 v56, v56\n\t"
        "v_exp_f32 v57, v57\n\t"
        "v_smfmac_f32_32x32x32_bf16 v[16:31], v[108:111], v[48:55], %[idx]\n\t"
        "v_exp_f32 v58, v58\n\t"
        "v_exp_f32 v59, v59\n\t"
        "ds_write_b128 v12, v[4:7] offset:1024\n\t"
        "v_exp_f32 v60, v60\n\t"
        "v_exp_f32 v61, v61\n\t"
        "v_exp_f32 v62, v62\n\t"
        "v_exp_f32 v63, v63\n\t"
        "v_exp_f32 v32, v32\n\t"
        "v_exp_f32 v33, v33\n\t"
        "v_smfmac_f32_32x32x32_bf16 v[16:31], v[112:115], v[56:63], %[idx]\n\t"
        "v_exp_f32 v34, v34\n\t"
        "v_exp_f32 v35, v35\n\t"
        "ds_write_b64 v12, v[8:9] offset:4096\n\t"
        "v_exp_f32 v36, v36\n\t"
        "v_mfma_f32_32x32x16_bf16 v[48:63], v[80:83], v[64:67], 0\n\t"
        "v_exp_f32 v37, v37\n\t"
        "v_exp_f32 v38, v38\n\t"
        "v_exp_f32 v39, v39\n\t"
        "v_exp_f32 v40, v40\n\t"
        "v_exp_f32 v41, v41\n\t"
        "v_smfmac_f32_32x32x32_bf16 v[16:31], v[116:119], v[32:39], %[idx]\n\t"
        "v_exp_f32 v42, v42\n\t"
        "v_exp_f32 v43, v43\n\t"
        "v_exp_f32 v44, v44\n\t"
        "v_exp_f32 v45, v45\n\t"
        "v_exp_f32 v46, v46\n\t"
        "v_exp_f32 v47, v47\n\t"
        "v_exp_f32 v48, v48\n\t"
        "v_exp_f32 v49, v49\n\t"
        "v_smfmac_f32_32x32x32_bf16 v[16:31], v[120:123], v[40:47], %[idx]\n\t"
        "v_exp_f32 v50, v50\n\t"
        "v_exp_f32 v51, v51\n\t"
        "v_exp_f32 v52, v52\n\t"
        "v_mfma_f32_32x32x16_bf16 v[32:47], v[84:87], v[64:67], 0\n\t"
        "v_exp_f32 v53, v53\n\t"
        "v_exp_f32 v54, v54\n\t"
        "v_exp_f32 v55, v55\n\t"
        "v_exp_f32 v56, v56\n\t"
        "v_exp_f32 v57, v57\n\t"
        "v_smfmac_f32_32x32x32_bf16 v[16:31], v[124:127], v[48:55], %[idx]\n\t"
        "v_exp_f32 v58, v58\n\t"
        "v_exp_f32 v59, v59\n\t"
        "v_exp_f32 v60, v60\n\t"
        "v_exp_f32 v61, v61\n\t"
        "v_exp_f32 v62, v62\n\t"
        "v_exp_f32 v63, v63\n\t"
        "v_exp_f32 v32, v32\n\t"
        "v_exp_f32 v33, v33\n\t"
        "v_smfmac_f32_32x32x32_bf16 v[16:31], v[128:131], v[56:63], %[idx]\n\t"
        "v_exp_f32 v34, v34\n\t"
        "v_exp_f32 v35, v35\n\t"
        "v_exp_f32 v36, v36\n\t"
        "v_mfma_f32_32x32x16_bf16 v[48:63], v[88:91], v[64:67], 0\n\t"
        "v_exp_f32 v37, v37\n\t"
        "v_exp_f32 v38, v38\n\t"
        "v_exp_f32 v39, v39\n\t"
        "v_exp_f32 v40, v40\n\t"
        "v_exp_f32 v41, v41\n\t"
        "v_smfmac_f32_32x32x32_bf16 v[16:31], v[132:135], v[32:39], %[idx]\n\t"
        "v_exp_f32 v42, v42\n\t"
        "v_exp_f32 v43, v43\n\t"
        "v_exp_f32 v44, v44\n\t"
        "v_exp_f32 v45, v45\n\t"
        "v_exp_f32 v46, v46\n\t"
        "v_exp_f32 v47, v47\n\t"
        "v_exp_f32 v48, v48\n\t"
        "v_exp_f32 v49, v49\n\t"
        "v_smfmac_f32_32x32x32_bf16 v[16:31], v[136:139], v[40:47], %[idx]\n\t"
        "v_exp_f32 v50, v50\n\t"
        "v_exp_f32 v51, v51\n\t"
        "v_exp_f32 v52, v52\n\t"
        "v_mfma_f32_32x32x16_bf16 v[32:47], v[92:95], v[64:67], 0\n\t"
        "v_exp_f32 v53, v53\n\t"
        "v_exp_f32 v54, v54\n\t"
        "v_exp_f32 v55, v55\n\t"
        "v_exp_f32 v56, v56\n\t"
        "v_exp_f32 v57, v57\n\t"
        "v_smfmac_f32_32x32x32_bf16 v[16:31], v[140:143], v[48:55], %[idx]\n\t"
        "v_exp_f32 v58, v58\n\t"
        "v_exp_f32 v59, v59\n\t"
        "v_add_u32 v12, %[so1], %[l16]\n\t"
        "v_exp_f32 v60, v60\n\t"
        "v_add_u32 v13, %[sq1], %[l16]\n\t"
        "v_exp_f32 v61, v61\n\t"
        "v_exp_f32 v62, v62\n\t"
        "v_exp_f32 v63, v63\n\t"
        "v_exp_f32 v32, v32\n\t"
        "v_exp_f32 v33, v33\n\t"
        "v_smfmac_f32_32x32x32_bf16 v[16:31], v[144:147], v[56:63], %[idx]\n\t"
        "v_exp_f32 v34, v34\n\t"
        "v_exp_f32 v35, v35\n\t"
        "v_exp_f32 v36, v36\n\t"
        "v_mfma_f32_32x32x16_bf16 v[48:63], v[96:99], v[64:67], 0\n\t"
        "v_exp_f32 v37, v37\n\t"
        "v_exp_f32 v38, v38\n\t"
        "ds_read_b64 v[8:9], v12 offset:4096\n\t"
        "ds_read_b128 v[0:3], v12 offset:0\n\t"
        "ds_read_b128 v[4:7], v12 offset:1024\n\t"
        "ds_read_b128 v[64:67], v13 offset:0\n\t"
        "v_exp_f32 v39, v39\n\t"
        "v_exp_f32 v40, v40\n\t"
        "v_exp_f32 v41, v41\n\t"
        "v_smfmac_f32_32x32x32_bf16 v[16:31], v[148:151], v[32:39], %[idx]\n\t"
        "v_exp_f32 v42, v42\n\t"
        "v_exp_f32 v43, v43\n\t"
        "v_exp_f32 v44, v44\n\t"
        "v_exp_f32 v45, v45\n\t"
        "v_exp_f32 v46, v46\n\t"
        "v_exp_f32 v47, v47\n\t"
        "v_exp_f32 v48, v48\n\t"
        "v_exp_f32 v49, v49\n\t"
        "v_smfmac_f32_32x32x32_bf16 v[16:31], v[152:155], v[40:47], %[idx]\n\t"
        "v_exp_f32 v50, v50\n\t"
        "s_waitcnt lgkmcnt(0)\n\t"
        "v_cmp_ne_u32 vcc, s41, v9\n\t"
        "v_exp_f32 v51, v51\n\t"
        "v_exp_f32 v52, v52\n\t"
        "v_mfma_f32_32x32x16_bf16 v[32:47], v[68:71], v[64:67], 0\n\t"
        "v_exp_f32 v53, v53\n\t"
        "v_exp_f32 v54, v54\n\t"
        "v_exp_f32 v55, v55\n\t"
        "v_exp_f32 v56, v56\n\t"
        "v_exp_f32 v57, v57\n\t"
        "v_smfmac_f32_32x32x32_bf16 v[16:31], v[156:159], v[48:55], %[idx]\n\t"
        "v_exp_f32 v58, v58\n\t"
        "v_exp_f32 v59, v59\n\t"
        "s_cbranch_vccnz .Lspin2_%=\n\t"
        ".Lspinret2_%=:\n\t"
        "v_exp_f32 v60, v60\n\t"
        "v_exp_f32 v61, v61\n\t"
        "v_exp_f32 v62, v62\n\t"
        "v_exp_f32 v63, v63\n\t"
        "v_exp_f32 v32, v32\n\t"
        "v_exp_f32 v33, v33\n\t"
        "v_smfmac_f32_32x32x32_bf16 v[16:31], v[160:163], v[56:63], %[idx]\n\t"
        "v_exp_f32 v34, v34\n\t"
        "v_exp_f32 v35, v35\n\t"
        "v_exp_f32 v36, v36\n\t"
        "v_mfma_f32_32x32x16_bf16 v[48:63], v[72:75], v[64:67], 0\n\t"
        "v_exp_f32 v37, v37\n\t"
        "v_exp_f32 v38, v38\n\t"
        "v_exp_f32 v39, v39\n\t"
        "v_exp_f32 v40, v40\n\t"
        "v_exp_f32 v41, v41\n\t"
        "v_smfmac_f32_32x32x32_bf16 v[0:15], v[100:103], v[32:39], %[idx]\n\t"
        "v_exp_f32 v42, v42\n\t"
        "v_exp_f32 v43, v43\n\t"
        "v_exp_f32 v44, v44\n\t"
        "v_exp_f32 v45, v45\n\t"
        "v_exp_f32 v46, v46\n\t"
        "v_exp_f32 v47, v47\n\t"
        "v_exp_f32 v48, v48\n\t"
        "v_add_u32 v28, %[so0], %[l16]\n\t"
        "v_mov_b32 v25, s41\n\t"
        "v_exp_f32 v49, v49\n\t"
        "v_smfmac_f32_32x32x32_bf16 v[0:15], v[104:107], v[40:47], %[idx]\n\t"
        "v_exp_f32 v50, v50\n\t"
        "ds_write_b128 v28, v[16:19] offset:2048\n\t"
        "v_exp_f32 v51, v51\n\t"
        "v_exp_f32 v52, v52\n\t"
        "v_mfma_f32_32x32x16_bf16 v[32:47], v[76:79], v[64:67], 0\n\t"
        "v_exp_f32 v53, v53\n\t"
        "v_exp_f32 v54, v54\n\t"
        "v_exp_f32 v55, v55\n\t"
        "v_exp_f32 v56, v56\n\t"
        "v_exp_f32 v57, v57\n\t"
        "v_smfmac_f32_32x32x32_bf16 v[0:15], v[108:111], v[48:55], %[idx]\n\t"
        "v_exp_f32 v58, v58\n\t"
        "v_exp_f32 v59, v59\n\t"
        "ds_write_b128 v28, v[20:23] offset:3072\n\t"
        "v_exp_f32 v60, v60\n\t"
        "v_exp_f32 v61, v61\n\t"
        "v_exp_f32 v62, v62\n\t"
        "v_exp_f32 v63, v63\n\t"
        "v_exp_f32 v32, v32\n\t"
        "v_exp_f32 v33, v33\n\t"
        "v_smfmac_f32_32x32x32_bf16 v[0:15], v[112:115], v[56:63], %[idx]\n\t"
        "v_exp_f32 v34, v34\n\t"
        "v_exp_f32 v35, v35\n\t"
        "ds_write_b64 v28, v[24:25] offset:4104\n\t"
        "v_exp_f32 v36, v36\n\t"
        "v_mfma_f32_32x32x16_bf16 v[48:63], v[80:83], v[64:67], 0\n\t"
        "v_exp_f32 v37, v37\n\t"
        "v_exp_f32 v38, v38\n\t"
        "v_exp_f32 v39, v39\n\t"
        "v_exp_f32 v40, v40\n\t"
        "v_exp_f32 v41, v41\n\t"
        "v_smfmac_f32_32x32x32_bf16 v[0:15], v[116:119], v[32:39], %[idx]\n\t"
        "v_exp_f32 v42, v42\n\t"
        "v_exp_f32 v43, v43\n\t"
        "v_exp_f32 v44, v44\n\t"
        "v_exp_f32 v45, v45\n\t"
        "v_exp_f32 v46, v46\n\t"
        "v_exp_f32 v47, v47\n\t"
        "v_exp_f32 v48, v48\n\t"
        "v_exp_f32 v49, v49\n\t"
        "v_smfmac_f32_32x32x32_bf16 v[0:15], v[120:123], v[40:47], %[idx]\n\t"
        "v_exp_f32 v50, v50\n\t"
        "v_exp_f32 v51, v51\n\t"
        "v_exp_f32 v52, v52\n\t"
        "v_mfma_f32_32x32x16_bf16 v[32:47], v[84:87], v[64:67], 0\n\t"
        "v_exp_f32 v53, v53\n\t"
        "v_exp_f32 v54, v54\n\t"
        "v_exp_f32 v55, v55\n\t"
        "v_exp_f32 v56, v56\n\t"
        "v_exp_f32 v57, v57\n\t"
        "v_smfmac_f32_32x32x32_bf16 v[0:15], v[124:127], v[48:55], %[idx]\n\t"
        "v_exp_f32 v58, v58\n\t"
        "v_exp_f32 v59, v59\n\t"
        "v_exp_f32 v60, v60\n\t"
        "v_exp_f32 v61, v61\n\t"
        "v_exp_f32 v62, v62\n\t"
        "v_exp_f32 v63, v63\n\t"
        "v_exp_f32 v32, v32\n\t"
        "v_exp_f32 v33, v33\n\t"
        "v_smfmac_f32_32x32x32_bf16 v[0:15], v[128:131], v[56:63], %[idx]\n\t"
        "v_exp_f32 v34, v34\n\t"
        "v_exp_f32 v35, v35\n\t"
        "v_exp_f32 v36, v36\n\t"
        "v_mfma_f32_32x32x16_bf16 v[48:63], v[88:91], v[64:67], 0\n\t"
        "v_exp_f32 v37, v37\n\t"
        "v_exp_f32 v38, v38\n\t"
        "v_exp_f32 v39, v39\n\t"
        "v_exp_f32 v40, v40\n\t"
        "v_exp_f32 v41, v41\n\t"
        "v_smfmac_f32_32x32x32_bf16 v[0:15], v[132:135], v[32:39], %[idx]\n\t"
        "v_exp_f32 v42, v42\n\t"
        "v_exp_f32 v43, v43\n\t"
        "v_exp_f32 v44, v44\n\t"
        "v_exp_f32 v45, v45\n\t"
        "v_exp_f32 v46, v46\n\t"
        "v_exp_f32 v47, v47\n\t"
        "v_exp_f32 v48, v48\n\t"
        "v_exp_f32 v49, v49\n\t"
        "v_smfmac_f32_32x32x32_bf16 v[0:15], v[136:139], v[40:47], %[idx]\n\t"
        "v_exp_f32 v50, v50\n\t"
        "v_exp_f32 v51, v51\n\t"
        "v_exp_f32 v52, v52\n\t"
        "v_mfma_f32_32x32x16_bf16 v[32:47], v[92:95], v[64:67], 0\n\t"
        "v_exp_f32 v53, v53\n\t"
        "v_exp_f32 v54, v54\n\t"
        "v_exp_f32 v55, v55\n\t"
        "v_exp_f32 v56, v56\n\t"
        "v_exp_f32 v57, v57\n\t"
        "v_smfmac_f32_32x32x32_bf16 v[0:15], v[140:143], v[48:55], %[idx]\n\t"
        "v_exp_f32 v58, v58\n\t"
        "v_exp_f32 v59, v59\n\t"
        "v_add_u32 v28, %[so1], %[l16]\n\t"
        "v_exp_f32 v60, v60\n\t"
        "v_add_u32 v29, %[sq1], %[l16]\n\t"
        "v_exp_f32 v61, v61\n\t"
        "v_exp_f32 v62, v62\n\t"
        "v_exp_f32 v63, v63\n\t"
        "v_exp_f32 v32, v32\n\t"
        "v_exp_f32 v33, v33\n\t"
        "v_smfmac_f32_32x32x32_bf16 v[0:15], v[144:147], v[56:63], %[idx]\n\t"
        "v_exp_f32 v34, v34\n\t"
        "v_exp_f32 v35, v35\n\t"
        "v_exp_f32 v36, v36\n\t"
        "v_mfma_f32_32x32x16_bf16 v[48:63], v[96:99], v[64:67], 0\n\t"
        "v_exp_f32 v37, v37\n\t"
        "v_exp_f32 v38, v38\n\t"
        "ds_read_b64 v[24:25], v28 offset:4104\n\t"
        "ds_read_b128 v[16:19], v28 offset:2048\n\t"
        "ds_read_b128 v[20:23], v28 offset:3072\n\t"
        "ds_read_b128 v[64:67], v29 offset:1024\n\t"
        "v_exp_f32 v39, v39\n\t"
        "v_exp_f32 v40, v40\n\t"
        "v_exp_f32 v41, v41\n\t"
        "v_smfmac_f32_32x32x32_bf16 v[0:15], v[148:151], v[32:39], %[idx]\n\t"
        "v_exp_f32 v42, v42\n\t"
        "v_exp_f32 v43, v43\n\t"
        "v_exp_f32 v44, v44\n\t"
        "v_exp_f32 v45, v45\n\t"
        "v_exp_f32 v46, v46\n\t"
        "v_exp_f32 v47, v47\n\t"
        "v_exp_f32 v48, v48\n\t"
        "v_exp_f32 v49, v49\n\t"
        "v_smfmac_f32_32x32x32_bf16 v[0:15], v[152:155], v[40:47], %[idx]\n\t"
        "v_exp_f32 v50, v50\n\t"
        "s_waitcnt lgkmcnt(0)\n\t"
        "v_cmp_ne_u32 vcc, s41, v25\n\t"
        "v_exp_f32 v51, v51\n\t"
        "v_exp_f32 v52, v52\n\t"
        "v_mfma_f32_32x32x16_bf16 v[32:47], v[68:71], v[64:67], 0\n\t"
        "v_exp_f32 v53, v53\n\t"
        "v_exp_f32 v54, v54\n\t"
        "v_exp_f32 v55, v55\n\t"
        "v_exp_f32 v56, v56\n\t"
        "v_exp_f32 v57, v57\n\t"
        "v_smfmac_f32_32x32x32_bf16 v[0:15], v[156:159], v[48:55], %[idx]\n\t"
        "v_exp_f32 v58, v58\n\t"
        "v_exp_f32 v59, v59\n\t"
        "s_cbranch_vccnz .Lspin3_%=\n\t"
        ".Lspinret3_%=:\n\t"
        "v_exp_f32 v60, v60\n\t"
        "v_exp_f32 v61, v61\n\t"
        "v_exp_f32 v62, v62\n\t"
        "v_exp_f32 v63, v63\n\t"
        "s_setprio 1\n\t"
        "v_exp_f32 v32, v32\n\t"
        "v_exp_f32 v33, v33\n\t"
        "v_smfmac_f32_32x32x32_bf16 v[0:15], v[160:163], v[56:63], %[idx]\n\t"
        "v_exp_f32 v34, v34\n\t"
        "v_exp_f32 v35, v35\n\t"
        "v_exp_f32 v36, v36\n\t"
        "v_mfma_f32_32x32x16_bf16 v[48:63], v[72:75], v[64:67], 0\n\t"
        "v_exp_f32 v37, v37\n\t"
        "v_exp_f32 v38, v38\n\t"
        "v_exp_f32 v39, v39\n\t"
        "v_exp_f32 v40, v40\n\t"
        "v_exp_f32 v41, v41\n\t"
        "v_smfmac_f32_32x32x32_bf16 v[16:31], v[100:103], v[32:39], %[idx]\n\t"
        "v_exp_f32 v42, v42\n\t"
        "v_exp_f32 v43, v43\n\t"
        "v_exp_f32 v44, v44\n\t"
        "v_exp_f32 v45, v45\n\t"
        "v_exp_f32 v46, v46\n\t"
        "v_exp_f32 v47, v47\n\t"
        "v_exp_f32 v48, v48\n\t"
        "v_add_u32 v12, %[so1], %[l16]\n\t"
        "v_mov_b32 v9, s42\n\t"
        "v_exp_f32 v49, v49\n\t"
        "v_smfmac_f32_32x32x32_bf16 v[16:31], v[104:107], v[40:47], %[idx]\n\t"
        "v_exp_f32 v50, v50\n\t"
        "ds_write_b128 v12, v[0:3] offset:0\n\t"
        "v_exp_f32 v51, v51\n\t"
        "v_exp_f32 v52, v52\n\t"
        "v_mfma_f32_32x32x16_bf16 v[32:47], v[76:79], v[64:67], 0\n\t"
        "v_exp_f32 v53, v53\n\t"
        "v_exp_f32 v54, v54\n\t"
        "v_exp_f32 v55, v55\n\t"
        "v_exp_f32 v56, v56\n\t"
        "v_exp_f32 v57, v57\n\t"
        "v_smfmac_f32_32x32x32_bf16 v[16:31], v[108:111], v[48:55], %[idx]\n\t"
        "v_exp_f32 v58, v58\n\t"
        "v_exp_f32 v59, v59\n\t"
        "ds_write_b128 v12, v[4:7] offset:1024\n\t"
        "v_exp_f32 v60, v60\n\t"
        "v_exp_f32 v61, v61\n\t"
        "v_exp_f32 v62, v62\n\t"
        "v_exp_f32 v63, v63\n\t"
        "v_exp_f32 v32, v32\n\t"
        "v_exp_f32 v33, v33\n\t"
        "v_smfmac_f32_32x32x32_bf16 v[16:31], v[112:115], v[56:63], %[idx]\n\t"
        "v_exp_f32 v34, v34\n\t"
        "v_exp_f32 v35, v35\n\t"
        "ds_write_b64 v12, v[8:9] offset:4096\n\t"
        "v_exp_f32 v36, v36\n\t"
        "v_mfma_f32_32x32x16_bf16 v[48:63], v[80:83], v[64:67], 0\n\t"
        "v_exp_f32 v37, v37\n\t"
        "v_exp_f32 v38, v38\n\t"
        "v_exp_f32 v39, v39\n\t"
        "v_exp_f32 v40, v40\n\t"
        "v_exp_f32 v41, v41\n\t"
        "v_smfmac_f32_32x32x32_bf16 v[16:31], v[116:119], v[32:39], %[idx]\n\t"
        "v_exp_f32 v42, v42\n\t"
        "v_exp_f32 v43, v43\n\t"
        "v_exp_f32 v44, v44\n\t"
        "v_exp_f32 v45, v45\n\t"
        "v_exp_f32 v46, v46\n\t"
        "v_exp_f32 v47, v47\n\t"
        "v_exp_f32 v48, v48\n\t"
        "v_exp_f32 v49, v49\n\t"
        "v_smfmac_f32_32x32x32_bf16 v[16:31], v[120:123], v[40:47], %[idx]\n\t"
        "v_exp_f32 v50, v50\n\t"
        "v_exp_f32 v51, v51\n\t"
        "v_exp_f32 v52, v52\n\t"
        "v_mfma_f32_32x32x16_bf16 v[32:47], v[84:87], v[64:67], 0\n\t"
        "v_exp_f32 v53, v53\n\t"
        "v_exp_f32 v54, v54\n\t"
        "v_exp_f32 v55, v55\n\t"
        "v_exp_f32 v56, v56\n\t"
        "v_exp_f32 v57, v57\n\t"
        "v_smfmac_f32_32x32x32_bf16 v[16:31], v[124:127], v[48:55], %[idx]\n\t"
        "v_exp_f32 v58, v58\n\t"
        "v_exp_f32 v59, v59\n\t"
        "v_exp_f32 v60, v60\n\t"
        "v_exp_f32 v61, v61\n\t"
        "v_exp_f32 v62, v62\n\t"
        "v_exp_f32 v63, v63\n\t"
        "v_exp_f32 v32, v32\n\t"
        "v_exp_f32 v33, v33\n\t"
        "v_smfmac_f32_32x32x32_bf16 v[16:31], v[128:131], v[56:63], %[idx]\n\t"
        "v_exp_f32 v34, v34\n\t"
        "v_exp_f32 v35, v35\n\t"
        "v_exp_f32 v36, v36\n\t"
        "v_mfma_f32_32x32x16_bf16 v[48:63], v[88:91], v[64:67], 0\n\t"
        "v_exp_f32 v37, v37\n\t"
        "v_exp_f32 v38, v38\n\t"
        "v_exp_f32 v39, v39\n\t"
        "v_exp_f32 v40, v40\n\t"
        "v_exp_f32 v41, v41\n\t"
        "v_smfmac_f32_32x32x32_bf16 v[16:31], v[132:135], v[32:39], %[idx]\n\t"
        "v_exp_f32 v42, v42\n\t"
        "v_exp_f32 v43, v43\n\t"
        "v_exp_f32 v44, v44\n\t"
        "v_exp_f32 v45, v45\n\t"
        "v_exp_f32 v46, v46\n\t"
        "v_exp_f32 v47, v47\n\t"
        "v_exp_f32 v48, v48\n\t"
        "v_exp_f32 v49, v49\n\t"
        "v_smfmac_f32_32x32x32_bf16 v[16:31], v[136:139], v[40:47], %[idx]\n\t"
        "v_exp_f32 v50, v50\n\t"
        "v_exp_f32 v51, v51\n\t"
        "v_exp_f32 v52, v52\n\t"
        "v_mfma_f32_32x32x16_bf16 v[32:47], v[92:95], v[64:67], 0\n\t"
        "v_exp_f32 v53, v53\n\t"
        "v_exp_f32 v54, v54\n\t"
        "v_exp_f32 v55, v55\n\t"
        "v_exp_f32 v56, v56\n\t"
        "v_exp_f32 v57, v57\n\t"
        "v_smfmac_f32_32x32x32_bf16 v[16:31], v[140:143], v[48:55], %[idx]\n\t"
        "v_exp_f32 v58, v58\n\t"
        "v_exp_f32 v59, v59\n\t"
        "v_add_u32 v12, %[so2], %[l16]\n\t"
        "v_exp_f32 v60, v60\n\t"
        "v_add_u32 v13, %[sq2], %[l16]\n\t"
        "v_exp_f32 v61, v61\n\t"
        "v_exp_f32 v62, v62\n\t"
        "v_exp_f32 v63, v63\n\t"
        "v_exp_f32 v32, v32\n\t"
        "v_exp_f32 v33, v33\n\t"
        "v_smfmac_f32_32x32x32_bf16 v[16:31], v[144:147], v[56:63], %[idx]\n\t"
        "v_exp_f32 v34, v34\n\t"
        "v_exp_f32 v35, v35\n\t"
        "v_exp_f32 v36, v36\n\t"
        "v_mfma_f32_32x32x16_bf16 v[48:63], v[96:99], v[64:67], 0\n\t"
        "v_exp_f32 v37, v37\n\t"
        "v_exp_f32 v38, v38\n\t"
        "ds_read_b64 v[8:9], v12 offset:4096\n\t"
        "ds_read_b128 v[0:3], v12 offset:0\n\t"
        "ds_read_b128 v[4:7], v12 offset:1024\n\t"
        "ds_read_b128 v[64:67], v13 offset:0\n\t"
        "v_exp_f32 v39, v39\n\t"
        "v_exp_f32 v40, v40\n\t"
        "v_exp_f32 v41, v41\n\t"
        "v_smfmac_f32_32x32x32_bf16 v[16:31], v[148:151], v[32:39], %[idx]\n\t"
        "v_exp_f32 v42, v42\n\t"
        "v_exp_f32 v43, v43\n\t"
        "v_exp_f32 v44, v44\n\t"
        "v_exp_f32 v45, v45\n\t"
        "v_exp_f32 v46, v46\n\t"
        "v_exp_f32 v47, v47\n\t"
        "v_exp_f32 v48, v48\n\t"
        "v_exp_f32 v49, v49\n\t"
        "v_smfmac_f32_32x32x32_bf16 v[16:31], v[152:155], v[40:47], %[idx]\n\t"
        "v_exp_f32 v50, v50\n\t"
        "s_waitcnt lgkmcnt(0)\n\t"
        "v_cmp_ne_u32 vcc, s42, v9\n\t"
        "v_exp_f32 v51, v51\n\t"
        "v_exp_f32 v52, v52\n\t"
        "v_mfma_f32_32x32x16_bf16 v[32:47], v[68:71], v[64:67], 0\n\t"
        "v_exp_f32 v53, v53\n\t"
        "v_exp_f32 v54, v54\n\t"
        "v_exp_f32 v55, v55\n\t"
        "v_exp_f32 v56, v56\n\t"
        "v_exp_f32 v57, v57\n\t"
        "v_smfmac_f32_32x32x32_bf16 v[16:31], v[156:159], v[48:55], %[idx]\n\t"
        "v_exp_f32 v58, v58\n\t"
        "v_exp_f32 v59, v59\n\t"
        "s_cbranch_vccnz .Lspin4_%=\n\t"
        ".Lspinret4_%=:\n\t"
        "v_exp_f32 v60, v60\n\t"
        "v_exp_f32 v61, v61\n\t"
        "v_exp_f32 v62, v62\n\t"
        "v_exp_f32 v63, v63\n\t"
        "v_exp_f32 v32, v32\n\t"
        "v_exp_f32 v33, v33\n\t"
        "v_smfmac_f32_32x32x32_bf16 v[16:31], v[160:163], v[56:63], %[idx]\n\t"
        "v_exp_f32 v34, v34\n\t"
        "v_exp_f32 v35, v35\n\t"
        "v_exp_f32 v36, v36\n\t"
        "v_mfma_f32_32x32x16_bf16 v[48:63], v[72:75], v[64:67], 0\n\t"
        "v_exp_f32 v37, v37\n\t"
        "v_exp_f32 v38, v38\n\t"
        "v_exp_f32 v39, v39\n\t"
        "v_exp_f32 v40, v40\n\t"
        "v_exp_f32 v41, v41\n\t"
        "v_smfmac_f32_32x32x32_bf16 v[0:15], v[100:103], v[32:39], %[idx]\n\t"
        "v_exp_f32 v42, v42\n\t"
        "v_exp_f32 v43, v43\n\t"
        "v_exp_f32 v44, v44\n\t"
        "v_exp_f32 v45, v45\n\t"
        "v_exp_f32 v46, v46\n\t"
        "v_exp_f32 v47, v47\n\t"
        "v_exp_f32 v48, v48\n\t"
        "v_add_u32 v28, %[so1], %[l16]\n\t"
        "v_mov_b32 v25, s42\n\t"
        "v_exp_f32 v49, v49\n\t"
        "v_smfmac_f32_32x32x32_bf16 v[0:15], v[104:107], v[40:47], %[idx]\n\t"
        "v_exp_f32 v50, v50\n\t"
        "ds_write_b128 v28, v[16:19] offset:2048\n\t"
        "v_exp_f32 v51, v51\n\t"
        "v_exp_f32 v52, v52\n\t"
        "v_mfma_f32_32x32x16_bf16 v[32:47], v[76:79], v[64:67], 0\n\t"
        "v_exp_f32 v53, v53\n\t"
        "v_exp_f32 v54, v54\n\t"
        "v_exp_f32 v55, v55\n\t"
        "v_exp_f32 v56, v56\n\t"
        "v_exp_f32 v57, v57\n\t"
        "v_smfmac_f32_32x32x32_bf16 v[0:15], v[108:111], v[48:55], %[idx]\n\t"
        "v_exp_f32 v58, v58\n\t"
        "v_exp_f32 v59, v59\n\t"
        "ds_write_b128 v28, v[20:23] offset:3072\n\t"
        "v_exp_f32 v60, v60\n\t"
        "v_exp_f32 v61, v61\n\t"
        "v_exp_f32 v62, v62\n\t"
        "v_exp_f32 v63, v63\n\t"
        "v_exp_f32 v32, v32\n\t"
        "v_exp_f32 v33, v33\n\t"
        "v_smfmac_f32_32x32x32_bf16 v[0:15], v[112:115], v[56:63], %[idx]\n\t"
        "v_exp_f32 v34, v34\n\t"
        "v_exp_f32 v35, v35\n\t"
        "ds_write_b64 v28, v[24:25] offset:4104\n\t"
        "v_exp_f32 v36, v36\n\t"
        "v_mfma_f32_32x32x16_bf16 v[48:63], v[80:83], v[64:67], 0\n\t"
        "v_exp_f32 v37, v37\n\t"
        "v_exp_f32 v38, v38\n\t"
        "v_exp_f32 v39, v39\n\t"
        "v_exp_f32 v40, v40\n\t"
        "v_exp_f32 v41, v41\n\t"
        "v_smfmac_f32_32x32x32_bf16 v[0:15], v[116:119], v[32:39], %[idx]\n\t"
        "v_exp_f32 v42, v42\n\t"
        "v_exp_f32 v43, v43\n\t"
        "v_exp_f32 v44, v44\n\t"
        "v_exp_f32 v45, v45\n\t"
        "v_exp_f32 v46, v46\n\t"
        "v_exp_f32 v47, v47\n\t"
        "v_exp_f32 v48, v48\n\t"
        "v_exp_f32 v49, v49\n\t"
        "v_smfmac_f32_32x32x32_bf16 v[0:15], v[120:123], v[40:47], %[idx]\n\t"
        "v_exp_f32 v50, v50\n\t"
        "v_exp_f32 v51, v51\n\t"
        "v_exp_f32 v52, v52\n\t"
        "v_mfma_f32_32x32x16_bf16 v[32:47], v[84:87], v[64:67], 0\n\t"
        "v_exp_f32 v53, v53\n\t"
        "v_exp_f32 v54, v54\n\t"
        "v_exp_f32 v55, v55\n\t"
        "v_exp_f32 v56, v56\n\t"
        "v_exp_f32 v57, v57\n\t"
        "v_smfmac_f32_32x32x32_bf16 v[0:15], v[124:127], v[48:55], %[idx]\n\t"
        "v_exp_f32 v58, v58\n\t"
        "v_exp_f32 v59, v59\n\t"
        "v_exp_f32 v60, v60\n\t"
        "v_exp_f32 v61, v61\n\t"
        "v_exp_f32 v62, v62\n\t"
        "v_exp_f32 v63, v63\n\t"
        "v_exp_f32 v32, v32\n\t"
        "v_exp_f32 v33, v33\n\t"
        "v_smfmac_f32_32x32x32_bf16 v[0:15], v[128:131], v[56:63], %[idx]\n\t"
        "v_exp_f32 v34, v34\n\t"
        "v_exp_f32 v35, v35\n\t"
        "v_exp_f32 v36, v36\n\t"
        "v_mfma_f32_32x32x16_bf16 v[48:63], v[88:91], v[64:67], 0\n\t"
        "v_exp_f32 v37, v37\n\t"
        "v_exp_f32 v38, v38\n\t"
        "v_exp_f32 v39, v39\n\t"
        "v_exp_f32 v40, v40\n\t"
        "v_exp_f32 v41, v41\n\t"
        "v_smfmac_f32_32x32x32_bf16 v[0:15], v[132:135], v[32:39], %[idx]\n\t"
        "v_exp_f32 v42, v42\n\t"
        "v_exp_f32 v43, v43\n\t"
        "v_exp_f32 v44, v44\n\t"
        "v_exp_f32 v45, v45\n\t"
        "v_exp_f32 v46, v46\n\t"
        "v_exp_f32 v47, v47\n\t"
        "v_exp_f32 v48, v48\n\t"
        "v_exp_f32 v49, v49\n\t"
        "v_smfmac_f32_32x32x32_bf16 v[0:15], v[136:139], v[40:47], %[idx]\n\t"
        "v_exp_f32 v50, v50\n\t"
        "v_exp_f32 v51, v51\n\t"
        "v_exp_f32 v52, v52\n\t"
        "v_mfma_f32_32x32x16_bf16 v[32:47], v[92:95], v[64:67], 0\n\t"
        "v_exp_f32 v53, v53\n\t"
        "v_exp_f32 v54, v54\n\t"
        "v_exp_f32 v55, v55\n\t"
        "v_exp_f32 v56, v56\n\t"
        "v_exp_f32 v57, v57\n\t"
        "v_smfmac_f32_32x32x32_bf16 v[0:15], v[140:143], v[48:55], %[idx]\n\t"
        "v_exp_f32 v58, v58\n\t"
        "v_exp_f32 v59, v59\n\t"
        "v_add_u32 v28, %[so2], %[l16]\n\t"
        "v_exp_f32 v60, v60\n\t"
        "v_add_u32 v29, %[sq2], %[l16]\n\t"
        "v_exp_f32 v61, v61\n\t"
        "v_exp_f32 v62, v62\n\t"
        "v_exp_f32 v63, v63\n\t"
        "v_exp_f32 v32, v32\n\t"
        "v_exp_f32 v33, v33\n\t"
        "v_smfmac_f32_32x32x32_bf16 v[0:15], v[144:147], v[56:63], %[idx]\n\t"
        "v_exp_f32 v34, v34\n\t"
        "v_exp_f32 v35, v35\n\t"
        "v_exp_f32 v36, v36\n\t"
        "v_mfma_f32_32x32x16_bf16 v[48:63], v[96:99], v[64:67], 0\n\t"
        "v_exp_f32 v37, v37\n\t"
        "v_exp_f32 v38, v38\n\t"
        "ds_read_b64 v[24:25], v28 offset:4104\n\t"
        "ds_read_b128 v[16:19], v28 offset:2048\n\t"
        "ds_read_b128 v[20:23], v28 offset:3072\n\t"
        "ds_read_b128 v[64:67], v29 offset:1024\n\t"
        "v_exp_f32 v39, v39\n\t"
        "v_exp_f32 v40, v40\n\t"
        "v_exp_f32 v41, v41\n\t"
        "v_smfmac_f32_32x32x32_bf16 v[0:15], v[148:151], v[32:39], %[idx]\n\t"
        "v_exp_f32 v42, v42\n\t"
        "v_exp_f32 v43, v43\n\t"
        "v_exp_f32 v44, v44\n\t"
        "v_exp_f32 v45, v45\n\t"
        "v_exp_f32 v46, v46\n\t"
        "v_exp_f32 v47, v47\n\t"
        "v_exp_f32 v48, v48\n\t"
        "v_exp_f32 v49, v49\n\t"
        "v_smfmac_f32_32x32x32_bf16 v[0:15], v[152:155], v[40:47], %[idx]\n\t"
        "v_exp_f32 v50, v50\n\t"
        "s_waitcnt lgkmcnt(0)\n\t"
        "v_cmp_ne_u32 vcc, s42, v25\n\t"
        "v_exp_f32 v51, v51\n\t"
        "v_exp_f32 v52, v52\n\t"
        "v_mfma_f32_32x32x16_bf16 v[32:47], v[68:71], v[64:67], 0\n\t"
        "v_exp_f32 v53, v53\n\t"
        "v_exp_f32 v54, v54\n\t"
        "v_exp_f32 v55, v55\n\t"
        "v_exp_f32 v56, v56\n\t"
        "v_exp_f32 v57, v57\n\t"
        "v_smfmac_f32_32x32x32_bf16 v[0:15], v[156:159], v[48:55], %[idx]\n\t"
        "v_exp_f32 v58, v58\n\t"
        "v_exp_f32 v59, v59\n\t"
        "s_cbranch_vccnz .Lspin5_%=\n\t"
        ".Lspinret5_%=:\n\t"
        "v_exp_f32 v60, v60\n\t"
        "v_exp_f32 v61, v61\n\t"
        "v_exp_f32 v62, v62\n\t"
        "v_exp_f32 v63, v63\n\t"
        "v_exp_f32 v32, v32\n\t"
        "v_exp_f32 v33, v33\n\t"
        "v_smfmac_f32_32x32x32_bf16 v[0:15], v[160:163], v[56:63], %[idx]\n\t"
        "v_exp_f32 v34, v34\n\t"
        "v_exp_f32 v35, v35\n\t"
        "v_exp_f32 v36, v36\n\t"
        "v_mfma_f32_32x32x16_bf16 v[48:63], v[72:75], v[64:67], 0\n\t"
        "v_exp_f32 v37, v37\n\t"
        "v_exp_f32 v38, v38\n\t"
        "v_exp_f32 v39, v39\n\t"
        "v_exp_f32 v40, v40\n\t"
        "v_exp_f32 v41, v41\n\t"
        "v_smfmac_f32_32x32x32_bf16 v[16:31], v[100:103], v[32:39], %[idx]\n\t"
        "v_exp_f32 v42, v42\n\t"
        "v_exp_f32 v43, v43\n\t"
        "v_exp_f32 v44, v44\n\t"
        "v_exp_f32 v45, v45\n\t"
        "v_exp_f32 v46, v46\n\t"
        "v_exp_f32 v47, v47\n\t"
        "v_exp_f32 v48, v48\n\t"
        "v_add_u32 v12, %[so2], %[l16]\n\t"
        "v_mov_b32 v9, s43\n\t"
        "v_exp_f32 v49, v49\n\t"
        "v_smfmac_f32_32x32x32_bf16 v[16:31], v[104:107], v[40:47], %[idx]\n\t"
        "v_exp_f32 v50, v50\n\t"
        "ds_write_b128 v12, v[0:3] offset:0\n\t"
        "v_exp_f32 v51, v51\n\t"
        "v_exp_f32 v52, v52\n\t"
        "v_mfma_f32_32x32x16_bf16 v[32:47], v[76:79], v[64:67], 0\n\t"
        "v_exp_f32 v53, v53\n\t"
        "v_exp_f32 v54, v54\n\t"
        "v_exp_f32 v55, v55\n\t"
        "v_exp_f32 v56, v56\n\t"
        "v_exp_f32 v57, v57\n\t"
        "v_smfmac_f32_32x32x32_bf16 v[16:31], v[108:111], v[48:55], %[idx]\n\t"
        "v_exp_f32 v58, v58\n\t"
        "v_exp_f32 v59, v59\n\t"
        "ds_write_b128 v12, v[4:7] offset:1024\n\t"
        "v_exp_f32 v60, v60\n\t"
        "v_exp_f32 v61, v61\n\t"
        "v_exp_f32 v62, v62\n\t"
        "v_exp_f32 v63, v63\n\t"
        "v_exp_f32 v32, v32\n\t"
        "v_exp_f32 v33, v33\n\t"
        "v_smfmac_f32_32x32x32_bf16 v[16:31], v[112:115], v[56:63], %[idx]\n\t"
        "v_exp_f32 v34, v34\n\t"
        "v_exp_f32 v35, v35\n\t"
        "ds_write_b64 v12, v[8:9] offset:4096\n\t"
        "v_exp_f32 v36, v36\n\t"
        "v_mfma_f32_32x32x16_bf16 v[48:63], v[80:83], v[64:67], 0\n\t"
        "v_exp_f32 v37, v37\n\t"
        "v_exp_f32 v38, v38\n\t"
        "v_exp_f32 v39, v39\n\t"
        "v_exp_f32 v40, v40\n\t"
        "v_exp_f32 v41, v41\n\t"
        "v_smfmac_f32_32x32x32_bf16 v[16:31], v[116:119], v[32:39], %[idx]\n\t"
        "v_exp_f32 v42, v42\n\t"
        "v_exp_f32 v43, v43\n\t"
        "v_exp_f32 v44, v44\n\t"
        "v_exp_f32 v45, v45\n\t"
        "v_exp_f32 v46, v46\n\t"
        "v_exp_f32 v47, v47\n\t"
        "v_exp_f32 v48, v48\n\t"
        "v_exp_f32 v49, v49\n\t"
        "v_smfmac_f32_32x32x32_bf16 v[16:31], v[120:123], v[40:47], %[idx]\n\t"
        "v_exp_f32 v50, v50\n\t"
        "v_exp_f32 v51, v51\n\t"
        "v_exp_f32 v52, v52\n\t"
        "v_mfma_f32_32x32x16_bf16 v[32:47], v[84:87], v[64:67], 0\n\t"
        "v_exp_f32 v53, v53\n\t"
        "v_exp_f32 v54, v54\n\t"
        "v_exp_f32 v55, v55\n\t"
        "v_exp_f32 v56, v56\n\t"
        "v_exp_f32 v57, v57\n\t"
        "v_smfmac_f32_32x32x32_bf16 v[16:31], v[124:127], v[48:55], %[idx]\n\t"
        "v_exp_f32 v58, v58\n\t"
        "v_exp_f32 v59, v59\n\t"
        "v_exp_f32 v60, v60\n\t"
        "v_exp_f32 v61, v61\n\t"
        "v_exp_f32 v62, v62\n\t"
        "v_exp_f32 v63, v63\n\t"
        "v_exp_f32 v32, v32\n\t"
        "v_exp_f32 v33, v33\n\t"
        "v_smfmac_f32_32x32x32_bf16 v[16:31], v[128:131], v[56:63], %[idx]\n\t"
        "v_exp_f32 v34, v34\n\t"
        "v_exp_f32 v35, v35\n\t"
        "v_exp_f32 v36, v36\n\t"
        "v_mfma_f32_32x32x16_bf16 v[48:63], v[88:91], v[64:67], 0\n\t"
        "v_exp_f32 v37, v37\n\t"
        "v_exp_f32 v38, v38\n\t"
        "v_exp_f32 v39, v39\n\t"
        "v_exp_f32 v40, v40\n\t"
        "v_exp_f32 v41, v41\n\t"
        "v_smfmac_f32_32x32x32_bf16 v[16:31], v[132:135], v[32:39], %[idx]\n\t"
        "v_exp_f32 v42, v42\n\t"
        "v_exp_f32 v43, v43\n\t"
        "v_exp_f32 v44, v44\n\t"
        "v_exp_f32 v45, v45\n\t"
        "v_exp_f32 v46, v46\n\t"
        "v_exp_f32 v47, v47\n\t"
        "v_exp_f32 v48, v48\n\t"
        "v_exp_f32 v49, v49\n\t"
        "v_smfmac_f32_32x32x32_bf16 v[16:31], v[136:139], v[40:47], %[idx]\n\t"
        "v_exp_f32 v50, v50\n\t"
        "v_exp_f32 v51, v51\n\t"
        "v_exp_f32 v52, v52\n\t"
        "v_mfma_f32_32x32x16_bf16 v[32:47], v[92:95], v[64:67], 0\n\t"
        "v_exp_f32 v53, v53\n\t"
        "v_exp_f32 v54, v54\n\t"
        "v_exp_f32 v55, v55\n\t"
        "v_exp_f32 v56, v56\n\t"
        "v_exp_f32 v57, v57\n\t"
        "v_smfmac_f32_32x32x32_bf16 v[16:31], v[140:143], v[48:55], %[idx]\n\t"
        "v_exp_f32 v58, v58\n\t"
        "v_exp_f32 v59, v59\n\t"
        "v_add_u32 v12, %[so3], %[l16]\n\t"
        "v_exp_f32 v60, v60\n\t"
        "v_add_u32 v13, %[sq3], %[l16]\n\t"
        "v_exp_f32 v61, v61\n\t"
        "v_exp_f32 v62, v62\n\t"
        "v_exp_f32 v63, v63\n\t"
        "v_exp_f32 v32, v32\n\t"
        "v_exp_f32 v33, v33\n\t"
        "v_smfmac_f32_32x32x32_bf16 v[16:31], v[144:147], v[56:63], %[idx]\n\t"
        "v_exp_f32 v34, v34\n\t"
        "v_exp_f32 v35, v35\n\t"
        "v_exp_f32 v36, v36\n\t"
        "v_mfma_f32_32x32x16_bf16 v[48:63], v[96:99], v[64:67], 0\n\t"
        "v_exp_f32 v37, v37\n\t"
        "v_exp_f32 v38, v38\n\t"
        "ds_read_b64 v[8:9], v12 offset:4096\n\t"
        "ds_read_b128 v[0:3], v12 offset:0\n\t"
        "ds_read_b128 v[4:7], v12 offset:1024\n\t"
        "ds_read_b128 v[64:67], v13 offset:0\n\t"
        "v_exp_f32 v39, v39\n\t"
        "v_exp_f32 v40, v40\n\t"
        "v_exp_f32 v41, v41\n\t"
        "v_smfmac_f32_32x32x32_bf16 v[16:31], v[148:151], v[32:39], %[idx]\n\t"
        "v_exp_f32 v42, v42\n\t"
        "v_exp_f32 v43, v43\n\t"
        "v_exp_f32 v44, v44\n\t"
        "v_exp_f32 v45, v45\n\t"
        "v_exp_f32 v46, v46\n\t"
        "v_exp_f32 v47, v47\n\t"
        "v_exp_f32 v48, v48\n\t"
        "v_exp_f32 v49, v49\n\t"
        "v_smfmac_f32_32x32x32_bf16 v[16:31], v[152:155], v[40:47], %[idx]\n\t"
        "v_exp_f32 v50, v50\n\t"
        "s_waitcnt lgkmcnt(0)\n\t"
        "v_cmp_ne_u32 vcc, s43, v9\n\t"
        "v_exp_f32 v51, v51\n\t"
        "v_exp_f32 v52, v52\n\t"
        "v_mfma_f32_32x32x16_bf16 v[32:47], v[68:71], v[64:67], 0\n\t"
        "v_exp_f32 v53, v53\n\t"
        "v_exp_f32 v54, v54\n\t"
        "v_exp_f32 v55, v55\n\t"
        "v_exp_f32 v56, v56\n\t"
        "v_exp_f32 v57, v57\n\t"
        "v_smfmac_f32_32x32x32_bf16 v[16:31], v[156:159], v[48:55], %[idx]\n\t"
        "v_exp_f32 v58, v58\n\t"
        "v_exp_f32 v59, v59\n\t"
        "s_cbranch_vccnz .Lspin6_%=\n\t"
        ".Lspinret6_%=:\n\t"
        "v_exp_f32 v60, v60\n\t"
        "v_exp_f32 v61, v61\n\t"
        "v_exp_f32 v62, v62\n\t"
        "v_exp_f32 v63, v63\n\t"
        "s_setprio 0\n\t"
        "v_exp_f32 v32, v32\n\t"
        "v_exp_f32 v33, v33\n\t"
        "v_smfmac_f32_32x32x32_bf16 v[16:31], v[160:163], v[56:63], %[idx]\n\t"
        "v_exp_f32 v34, v34\n\t"
        "v_exp_f32 v35, v35\n\t"
        "v_exp_f32 v36, v36\n\t"
        "v_mfma_f32_32x32x16_bf16 v[48:63], v[72:75], v[64:67], 0\n\t"
        "v_exp_f32 v37, v37\n\t"
        "v_exp_f32 v38, v38\n\t"
        "v_exp_f32 v39, v39\n\t"
        "v_exp_f32 v40, v40\n\t"
        "v_exp_f32 v41, v41\n\t"
        "v_smfmac_f32_32x32x32_bf16 v[0:15], v[100:103], v[32:39], %[idx]\n\t"
        "v_exp_f32 v42, v42\n\t"
        "v_exp_f32 v43, v43\n\t"
        "v_exp_f32 v44, v44\n\t"
        "v_exp_f32 v45, v45\n\t"
        "v_exp_f32 v46, v46\n\t"
        "v_exp_f32 v47, v47\n\t"
        "v_exp_f32 v48, v48\n\t"
        "v_add_u32 v28, %[so2], %[l16]\n\t"
        "v_mov_b32 v25, s43\n\t"
        "v_exp_f32 v49, v49\n\t"
        "v_smfmac_f32_32x32x32_bf16 v[0:15], v[104:107], v[40:47], %[idx]\n\t"
        "v_exp_f32 v50, v50\n\t"
        "ds_write_b128 v28, v[16:19] offset:2048\n\t"
        "v_exp_f32 v51, v51\n\t"
        "v_exp_f32 v52, v52\n\t"
        "v_mfma_f32_32x32x16_bf16 v[32:47], v[76:79], v[64:67], 0\n\t"
        "v_exp_f32 v53, v53\n\t"
        "v_exp_f32 v54, v54\n\t"
        "v_exp_f32 v55, v55\n\t"
        "v_exp_f32 v56, v56\n\t"
        "v_exp_f32 v57, v57\n\t"
        "v_smfmac_f32_32x32x32_bf16 v[0:15], v[108:111], v[48:55], %[idx]\n\t"
        "v_exp_f32 v58, v58\n\t"
        "v_exp_f32 v59, v59\n\t"
        "ds_write_b128 v28, v[20:23] offset:3072\n\t"
        "v_exp_f32 v60, v60\n\t"
        "v_exp_f32 v61, v61\n\t"
        "v_exp_f32 v62, v62\n\t"
        "v_exp_f32 v63, v63\n\t"
        "v_exp_f32 v32, v32\n\t"
        "v_exp_f32 v33, v33\n\t"
        "v_smfmac_f32_32x32x32_bf16 v[0:15], v[112:115], v[56:63], %[idx]\n\t"
        "v_exp_f32 v34, v34\n\t"
        "v_exp_f32 v35, v35\n\t"
        "ds_write_b64 v28, v[24:25] offset:4104\n\t"
        "v_exp_f32 v36, v36\n\t"
        "v_mfma_f32_32x32x16_bf16 v[48:63], v[80:83], v[64:67], 0\n\t"
        "v_exp_f32 v37, v37\n\t"
        "v_exp_f32 v38, v38\n\t"
        "v_exp_f32 v39, v39\n\t"
        "v_exp_f32 v40, v40\n\t"
        "v_exp_f32 v41, v41\n\t"
        "v_smfmac_f32_32x32x32_bf16 v[0:15], v[116:119], v[32:39], %[idx]\n\t"
        "v_exp_f32 v42, v42\n\t"
        "v_exp_f32 v43, v43\n\t"
        "v_exp_f32 v44, v44\n\t"
        "v_exp_f32 v45, v45\n\t"
        "v_exp_f32 v46, v46\n\t"
        "v_exp_f32 v47, v47\n\t"
        "v_exp_f32 v48, v48\n\t"
        "v_exp_f32 v49, v49\n\t"
        "v_smfmac_f32_32x32x32_bf16 v[0:15], v[120:123], v[40:47], %[idx]\n\t"
        "v_exp_f32 v50, v50\n\t"
        "v_exp_f32 v51, v51\n\t"
        "v_exp_f32 v52, v52\n\t"
        "v_mfma_f32_32x32x16_bf16 v[32:47], v[84:87], v[64:67], 0\n\t"
        "v_exp_f32 v53, v53\n\t"
        "v_exp_f32 v54, v54\n\t"
        "v_exp_f32 v55, v55\n\t"
        "v_exp_f32 v56, v56\n\t"
        "v_exp_f32 v57, v57\n\t"
        "v_smfmac_f32_32x32x32_bf16 v[0:15], v[124:127], v[48:55], %[idx]\n\t"
        "v_exp_f32 v58, v58\n\t"
        "v_exp_f32 v59, v59\n\t"
        "v_exp_f32 v60, v60\n\t"
        "v_exp_f32 v61, v61\n\t"
        "v_exp_f32 v62, v62\n\t"
        "v_exp_f32 v63, v63\n\t"
        "v_exp_f32 v32, v32\n\t"
        "v_exp_f32 v33, v33\n\t"
        "v_smfmac_f32_32x32x32_bf16 v[0:15], v[128:131], v[56:63], %[idx]\n\t"
        "v_exp_f32 v34, v34\n\t"
        "v_exp_f32 v35, v35\n\t"
        "v_exp_f32 v36, v36\n\t"
        "v_mfma_f32_32x32x16_bf16 v[48:63], v[88:91], v[64:67], 0\n\t"
        "v_exp_f32 v37, v37\n\t"
        "v_exp_f32 v38, v38\n\t"
        "v_exp_f32 v39, v39\n\t"
        "v_exp_f32 v40, v40\n\t"
        "v_exp_f32 v41, v41\n\t"
        "v_smfmac_f32_32x32x32_bf16 v[0:15], v[132:135], v[32:39], %[idx]\n\t"
        "v_exp_f32 v42, v42\n\t"
        "v_exp_f32 v43, v43\n\t"
        "v_exp_f32 v44, v44\n\t"
        "v_exp_f32 v45, v45\n\t"
        "v_exp_f32 v46, v46\n\t"
        "v_exp_f32 v47, v47\n\t"
        "v_exp_f32 v48, v48\n\t"
        "v_exp_f32 v49, v49\n\t"
        "v_smfmac_f32_32x32x32_bf16 v[0:15], v[136:139], v[40:47], %[idx]\n\t"
        "v_exp_f32 v50, v50\n\t"
        "v_exp_f32 v51, v51\n\t"
        "v_exp_f32 v52, v52\n\t"
        "v_mfma_f32_32x32x16_bf16 v[32:47], v[92:95], v[64:67], 0\n\t"
        "v_exp_f32 v53, v53\n\t"
        "v_exp_f32 v54, v54\n\t"
        "v_exp_f32 v55, v55\n\t"
        "v_exp_f32 v56, v56\n\t"
        "v_exp_f32 v57, v57\n\t"
        "v_smfmac_f32_32x32x32_bf16 v[0:15], v[140:143], v[48:55], %[idx]\n\t"
        "v_exp_f32 v58, v58\n\t"
        "v_exp_f32 v59, v59\n\t"
        "v_add_u32 v28, %[so3], %[l16]\n\t"
        "v_exp_f32 v60, v60\n\t"
        "v_add_u32 v29, %[sq3], %[l16]\n\t"
        "v_exp_f32 v61, v61\n\t"
        "v_exp_f32 v62, v62\n\t"
        "v_exp_f32 v63, v63\n\t"
        "v_exp_f32 v32, v32\n\t"
        "v_exp_f32 v33, v33\n\t"
        "v_smfmac_f32_32x32x32_bf16 v[0:15], v[144:147], v[56:63], %[idx]\n\t"
        "v_exp_f32 v34, v34\n\t"
        "v_exp_f32 v35, v35\n\t"
        "v_exp_f32 v36, v36\n\t"
        "v_mfma_f32_32x32x16_bf16 v[48:63], v[96:99], v[64:67], 0\n\t"
        "v_exp_f32 v37, v37\n\t"
        "v_exp_f32 v38, v38\n\t"
        "ds_read_b64 v[24:25], v28 offset:4104\n\t"
        "ds_read_b128 v[16:19], v28 offset:2048\n\t"
        "ds_read_b128 v[20:23], v28 offset:3072\n\t"
        "ds_read_b128 v[64:67], v29 offset:1024\n\t"
        "v_exp_f32 v39, v39\n\t"
        "v_exp_f32 v40, v40\n\t"
        "v_exp_f32 v41, v41\n\t"
        "v_smfmac_f32_32x32x32_bf16 v[0:15], v[148:151], v[32:39], %[idx]\n\t"
        "v_exp_f32 v42, v42\n\t"
        "v_exp_f32 v43, v43\n\t"
        "v_exp_f32 v44, v44\n\t"
        "v_exp_f32 v45, v45\n\t"
        "v_exp_f32 v46, v46\n\t"
        "v_exp_f32 v47, v47\n\t"
        "v_exp_f32 v48, v48\n\t"
        "v_exp_f32 v49, v49\n\t"
        "v_smfmac_f32_32x32x32_bf16 v[0:15], v[152:155], v[40:47], %[idx]\n\t"
        "v_exp_f32 v50, v50\n\t"
        "s_waitcnt lgkmcnt(0)\n\t"
        "v_cmp_ne_u32 vcc, s43, v25\n\t"
        "v_exp_f32 v51, v51\n\t"
        "v_exp_f32 v52, v52\n\t"
        "v_mfma_f32_32x32x16_bf16 v[32:47], v[68:71], v[64:67], 0\n\t"
        "v_exp_f32 v53, v53\n\t"
        "v_exp_f32 v54, v54\n\t"
        "v_exp_f32 v55, v55\n\t"
        "v_exp_f32 v56, v56\n\t"
        "v_exp_f32 v57, v57\n\t"
        "v_smfmac_f32_32x32x32_bf16 v[0:15], v[156:159], v[48:55], %[idx]\n\t"
        "v_exp_f32 v58, v58\n\t"
        "v_exp_f32 v59, v59\n\t"
        "s_cbranch_vccnz .Lspin7_%=\n\t"
        ".Lspinret7_%=:\n\t"
        "v_exp_f32 v60, v60\n\t"
        "v_exp_f32 v61, v61\n\t"
        "v_exp_f32 v62, v62\n\t"
        "v_exp_f32 v63, v63\n\t"
        "v_exp_f32 v32, v32\n\t"
        "v_exp_f32 v33, v33\n\t"
        "v_smfmac_f32_32x32x32_bf16 v[0:15], v[160:163], v[56:63], %[idx]\n\t"
        "v_exp_f32 v34, v34\n\t"
        "v_exp_f32 v35, v35\n\t"
        "v_exp_f32 v36, v36\n\t"
        "v_mfma_f32_32x32x16_bf16 v[48:63], v[72:75], v[64:67], 0\n\t"
        "v_exp_f32 v37, v37\n\t"
        "v_exp_f32 v38, v38\n\t"
        "v_exp_f32 v39, v39\n\t"
        "v_exp_f32 v40, v40\n\t"
        "v_exp_f32 v41, v41\n\t"
        "v_smfmac_f32_32x32x32_bf16 v[16:31], v[100:103], v[32:39], %[idx]\n\t"
        "v_exp_f32 v42, v42\n\t"
        "v_exp_f32 v43, v43\n\t"
        "v_exp_f32 v44, v44\n\t"
        "v_exp_f32 v45, v45\n\t"
        "v_exp_f32 v46, v46\n\t"
        "v_exp_f32 v47, v47\n\t"
        "v_exp_f32 v48, v48\n\t"
        "v_add_u32 v12, %[so3], %[l16]\n\t"
        "v_mov_b32 v9, s44\n\t"
        "v_exp_f32 v49, v49\n\t"
        "v_smfmac_f32_32x32x32_bf16 v[16:31], v[104:107], v[40:47], %[idx]\n\t"
        "v_exp_f32 v50, v50\n\t"
        "ds_write_b128 v12, v[0:3] offset:0\n\t"
        "v_exp_f32 v51, v51\n\t"
        "v_exp_f32 v52, v52\n\t"
        "v_mfma_f32_32x32x16_bf16 v[32:47], v[76:79], v[64:67], 0\n\t"
        "v_exp_f32 v53, v53\n\t"
        "v_exp_f32 v54, v54\n\t"
        "v_exp_f32 v55, v55\n\t"
        "v_exp_f32 v56, v56\n\t"
        "v_exp_f32 v57, v57\n\t"
        "v_smfmac_f32_32x32x32_bf16 v[16:31], v[108:111], v[48:55], %[idx]\n\t"
        "v_exp_f32 v58, v58\n\t"
        "v_exp_f32 v59, v59\n\t"
        "ds_write_b128 v12, v[4:7] offset:1024\n\t"
        "v_exp_f32 v60, v60\n\t"
        "v_exp_f32 v61, v61\n\t"
        "v_exp_f32 v62, v62\n\t"
        "v_exp_f32 v63, v63\n\t"
        "v_exp_f32 v32, v32\n\t"
        "v_exp_f32 v33, v33\n\t"
        "v_smfmac_f32_32x32x32_bf16 v[16:31], v[112:115], v[56:63], %[idx]\n\t"
        "v_exp_f32 v34, v34\n\t"
        "v_exp_f32 v35, v35\n\t"
        "ds_write_b64 v12, v[8:9] offset:4096\n\t"
        "v_exp_f32 v36, v36\n\t"
        "v_mfma_f32_32x32x16_bf16 v[48:63], v[80:83], v[64:67], 0\n\t"
        "v_exp_f32 v37, v37\n\t"
        "v_exp_f32 v38, v38\n\t"
        "v_exp_f32 v39, v39\n\t"
        "v_exp_f32 v40, v40\n\t"
        "v_exp_f32 v41, v41\n\t"
        "v_smfmac_f32_32x32x32_bf16 v[16:31], v[116:119], v[32:39], %[idx]\n\t"
        "v_exp_f32 v42, v42\n\t"
        "v_exp_f32 v43, v43\n\t"
        "v_exp_f32 v44, v44\n\t"
        "v_exp_f32 v45, v45\n\t"
        "v_exp_f32 v46, v46\n\t"
        "v_exp_f32 v47, v47\n\t"
        "v_exp_f32 v48, v48\n\t"
        "v_exp_f32 v49, v49\n\t"
        "v_smfmac_f32_32x32x32_bf16 v[16:31], v[120:123], v[40:47], %[idx]\n\t"
        "v_exp_f32 v50, v50\n\t"
        "v_exp_f32 v51, v51\n\t"
        "v_exp_f32 v52, v52\n\t"
        "v_mfma_f32_32x32x16_bf16 v[32:47], v[84:87], v[64:67], 0\n\t"
        "v_exp_f32 v53, v53\n\t"
        "v_exp_f32 v54, v54\n\t"
        "v_exp_f32 v55, v55\n\t"
        "v_exp_f32 v56, v56\n\t"
        "v_exp_f32 v57, v57\n\t"
        "v_smfmac_f32_32x32x32_bf16 v[16:31], v[124:127], v[48:55], %[idx]\n\t"
        "v_exp_f32 v58, v58\n\t"
        "v_exp_f32 v59, v59\n\t"
        "v_exp_f32 v60, v60\n\t"
        "v_exp_f32 v61, v61\n\t"
        "v_exp_f32 v62, v62\n\t"
        "v_exp_f32 v63, v63\n\t"
        "v_exp_f32 v32, v32\n\t"
        "v_exp_f32 v33, v33\n\t"
        "v_smfmac_f32_32x32x32_bf16 v[16:31], v[128:131], v[56:63], %[idx]\n\t"
        "v_exp_f32 v34, v34\n\t"
        "v_exp_f32 v35, v35\n\t"
        "v_exp_f32 v36, v36\n\t"
        "v_mfma_f32_32x32x16_bf16 v[48:63], v[88:91], v[64:67], 0\n\t"
        "v_exp_f32 v37, v37\n\t"
        "v_exp_f32 v38, v38\n\t"
        "v_exp_f32 v39, v39\n\t"
        "v_exp_f32 v40, v40\n\t"
        "v_exp_f32 v41, v41\n\t"
        "v_smfmac_f32_32x32x32_bf16 v[16:31], v[132:135], v[32:39], %[idx]\n\t"
        "v_exp_f32 v42, v42\n\t"
        "v_exp_f32 v43, v43\n\t"
        "v_exp_f32 v44, v44\n\t"
        "v_exp_f32 v45, v45\n\t"
        "v_exp_f32 v46, v46\n\t"
        "v_exp_f32 v47, v47\n\t"
        "v_exp_f32 v48, v48\n\t"
        "v_exp_f32 v49, v49\n\t"
        "v_smfmac_f32_32x32x32_bf16 v[16:31], v[136:139], v[40:47], %[idx]\n\t"
        "v_exp_f32 v50, v50\n\t"
        "v_exp_f32 v51, v51\n\t"
        "v_exp_f32 v52, v52\n\t"
        "v_mfma_f32_32x32x16_bf16 v[32:47], v[92:95], v[64:67], 0\n\t"
        "v_exp_f32 v53, v53\n\t"
        "v_exp_f32 v54, v54\n\t"
        "v_exp_f32 v55, v55\n\t"
        "v_exp_f32 v56, v56\n\t"
        "v_exp_f32 v57, v57\n\t"
        "v_smfmac_f32_32x32x32_bf16 v[16:31], v[140:143], v[48:55], %[idx]\n\t"
        "v_exp_f32 v58, v58\n\t"
        "v_exp_f32 v59, v59\n\t"
        "v_add_u32 v12, %[sx], %[l16]\n\t"
        "v_exp_f32 v60, v60\n\t"
        "v_mov_b32 v15, %[c9]\n\t"
        "v_and_b32 v14, 0x1f0, %[l16]\n\t"
        "v_lshrrev_b32 v14, 2, v14\n\t"
        "v_add_u32 v14, %[sg], v14\n\t"
        "v_exp_f32 v61, v61\n\t"
        "v_exp_f32 v62, v62\n\t"
        "v_exp_f32 v63, v63\n\t"
        "v_exp_f32 v32, v32\n\t"
        "v_exp_f32 v33, v33\n\t"
        "v_smfmac_f32_32x32x32_bf16 v[16:31], v[144:147], v[56:63], %[idx]\n\t"
        "v_exp_f32 v34, v34\n\t"
        "v_exp_f32 v35, v35\n\t"
        "v_exp_f32 v36, v36\n\t"
        "v_mfma_f32_32x32x16_bf16 v[48:63], v[96:99], v[64:67], 0\n\t"
        "v_exp_f32 v37, v37\n\t"
        "v_exp_f32 v38, v38\n\t"
        "ds_read_b64 v[8:9], v12 offset:4104\n\t"
        "ds_read_b128 v[0:3], v12 offset:2048\n\t"
        "ds_read_b128 v[4:7], v12 offset:3072\n\t"
        "ds_read_b32 v15, v15\n\t"
        "ds_read_b32 v14, v14\n\t"
        "v_exp_f32 v39, v39\n\t"
        "v_exp_f32 v40, v40\n\t"
        "v_exp_f32 v41, v41\n\t"
        "v_smfmac_f32_32x32x32_bf16 v[16:31], v[148:151], v[32:39], %[idx]\n\t"
        "v_exp_f32 v42, v42\n\t"
        "v_exp_f32 v43, v43\n\t"
        "v_exp_f32 v44, v44\n\t"
        "v_exp_f32 v45, v45\n\t"
        "v_exp_f32 v46, v46\n\t"
        "v_exp_f32 v47, v47\n\t"
        "v_exp_f32 v48, v48\n\t"
        "v_exp_f32 v49, v49\n\t"
        "v_smfmac_f32_32x32x32_bf16 v[16:31], v[152:155], v[40:47], %[idx]\n\t"
        "v_exp_f32 v50, v50\n\t"
        "s_waitcnt lgkmcnt(0)\n\t"
        "v_cmp_ne_u32 vcc, s44, v9\n\t"
        "v_exp_f32 v51, v51\n\t"
        "v_exp_f32 v52, v52\n\t"
        "v_exp_f32 v53, v53\n\t"
        "v_exp_f32 v54, v54\n\t"
        "v_exp_f32 v55, v55\n\t"
        "v_exp_f32 v56, v56\n\t"
        "v_exp_f32 v57, v57\n\t"
        "v_smfmac_f32_32x32x32_bf16 v[16:31], v[156:159], v[48:55], %[idx]\n\t"
        "v_exp_f32 v58, v58\n\t"
        "s_and_b64 vcc, vcc, s[46:47]\n\t"
        "v_exp_f32 v59, v59\n\t"
        "s_cbranch_vccnz .Lspin8_%=\n\t"
        ".Lspinret8_%=:\n\t"
        "v_exp_f32 v60, v60\n\t"
        "v_exp_f32 v61, v61\n\t"
        "v_exp_f32 v62, v62\n\t"
        "v_exp_f32 v63, v63\n\t"
        "s_nop 1\n\t"
        "v_smfmac_f32_32x32x32_bf16 v[16:31], v[160:163], v[56:63], %[idx]\n\t"
        "s_setprio 3\n\t"
        "s_nop 7\n\t"
        "s_nop 2\n\t"
        "s_branch .Lend_%=\n\t"
        ".Lspin2_%=:\n\t"
        "s_sleep 1\n\t"
        "ds_read_b64 v[8:9], v12 offset:4096\n\t"
        "ds_read_b128 v[0:3], v12 offset:0\n\t"
        "ds_read_b128 v[4:7], v12 offset:1024\n\t"
        "s_waitcnt lgkmcnt(0)\n\t"
        "v_cmp_ne_u32 vcc, s41, v9\n\t"
        "s_cbranch_vccz .Lspinret2_%=\n\t"
        "s_sub_u32 s45, s45, 1\n\t"
        "s_cbranch_scc0 .Lspin2_%=\n\t"
        "s_mov_b32 s45, 0\n\t"
        "s_branch .Lspinret2_%=\n\t"
        ".Lspin3_%=:\n\t"
        "s_sleep 1\n\t"
        "ds_read_b64 v[24:25], v28 offset:4104\n\t"
        "ds_read_b128 v[16:19], v28 offset:2048\n\t"
        "ds_read_b128 v[20:23], v28 offset:3072\n\t"
        "s_waitcnt lgkmcnt(0)\n\t"
        "v_cmp_ne_u32 vcc, s41, v25\n\t"
        "s_cbranch_vccz .Lspinret3_%=\n\t"
        "s_sub_u32 s45, s45, 1\n\t"
        "s_cbranch_scc0 .Lspin3_%=\n\t"
        "s_mov_b32 s45, 0\n\t"
        "s_branch .Lspinret3_%=\n\t"
        ".Lspin4_%=:\n\t"
        "s_sleep 1\n\t"
        "ds_read_b64 v[8:9], v12 offset:4096\n\t"
        "ds_read_b128 v[0:3], v12 offset:0\n\t"
        "ds_read_b128 v[4:7], v12 offset:1024\n\t"
        "s_waitcnt lgkmcnt(0)\n\t"
        "v_cmp_ne_u32 vcc, s42, v9\n\t"
        "s_cbranch_vccz .Lspinret4_%=\n\t"
        "s_sub_u32 s45, s45, 1\n\t"
        "s_cbranch_scc0 .Lspin4_%=\n\t"
        "s_mov_b32 s45, 0\n\t"
        "s_branch .Lspinret4_%=\n\t"
        ".Lspin5_%=:\n\t"
        "s_sleep 1\n\t"
        "ds_read_b64 v[24:25], v28 offset:4104\n\t"
        "ds_read_b128 v[16:19], v28 offset:2048\n\t"
        "ds_read_b128 v[20:23], v28 offset:3072\n\t"
        "s_waitcnt lgkmcnt(0)\n\t"
        "v_cmp_ne_u32 vcc, s42, v25\n\t"
        "s_cbranch_vccz .Lspinret5_%=\n\t"
        "s_sub_u32 s45, s45, 1\n\t"
        "s_cbranch_scc0 .Lspin5_%=\n\t"
        "s_mov_b32 s45, 0\n\t"
        "s_branch .Lspinret5_%=\n\t"
        ".Lspin6_%=:\n\t"
        "s_sleep 1\n\t"
        "ds_read_b64 v[8:9], v12 offset:4096\n\t"
        "ds_read_b128 v[0:3], v12 offset:0\n\t"
        "ds_read_b128 v[4:7], v12 offset:1024\n\t"
        "s_waitcnt lgkmcnt(0)\n\t"
        "v_cmp_ne_u32 vcc, s43, v9\n\t"
        "s_cbranch_vccz .Lspinret6_%=\n\t"
        "s_sub_u32 s45, s45, 1\n\t"
        "s_cbranch_scc0 .Lspin6_%=\n\t"
        "s_mov_b32 s45, 0\n\t"
        "s_branch .Lspinret6_%=\n\t"
        ".Lspin7_%=:\n\t"
        "s_sleep 1\n\t"
        "ds_read_b64 v[24:25], v28 offset:4104\n\t"
        "ds_read_b128 v[16:19], v28 offset:2048\n\t"
        "ds_read_b128 v[20:23], v28 offset:3072\n\t"
        "s_waitcnt lgkmcnt(0)\n\t"
        "v_cmp_ne_u32 vcc, s43, v25\n\t"
        "s_cbranch_vccz .Lspinret7_%=\n\t"
        "s_sub_u32 s45, s45, 1\n\t"
        "s_cbranch_scc0 .Lspin7_%=\n\t"
        "s_mov_b32 s45, 0\n\t"
        "s_branch .Lspinret7_%=\n\t"
        ".Lspin8_%=:\n\t"
        "s_sleep 1\n\t"
        "ds_read_b64 v[8:9], v12 offset:4104\n\t"
        "ds_read_b128 v[0:3], v12 offset:2048\n\t"
        "ds_read_b128 v[4:7], v12 offset:3072\n\t"
        "s_waitcnt lgkmcnt(0)\n\t"
        "v_cmp_ne_u32 vcc, s44, v9\n\t"
        "s_cbranch_vccz .Lspinret8_%=\n\t"
        "s_sub_u32 s45, s45, 1\n\t"
        "s_cbranch_scc0 .Lspin8_%=\n\t"
        "s_mov_b32 s45, 0\n\t"
        "s_branch .Lspinret8_%=\n\t"
        ".Lend_%=:\n\t"
        : "+{v[0:15]}"(O0), "+{v[16:31]}"(O1),
          "+{v[68:83]}"(r.k0), "+{v[84:99]}"(r.k1), "+{v[100:115]}"(r.v0), "+{v[116:131]}"(r.v1), "+{v[132:147]}"(r.v2), "+{v[148:163]}"(r.v3)
        : [l16] "v"(l16), [idx] "v"(0xDDDD), [vb] "s"(vb), [second] "s"(second),
          [so0] "s"(os + (pos & 3) * PAIR), [so1] "s"(os + ((pos + 1) & 3) * PAIR), [so2] "s"(os + ((pos + 2) & 3) * PAIR), [so3] "s"(os + ((pos + 3) & 3) * PAIR),
          [sq0] "s"(qs + (pos & 3) * 2048), [sq1] "s"(qs + ((pos + 1) & 3) * 2048), [sq2] "s"(qs + ((pos + 2) & 3) * 2048), [sq3] "s"(qs + ((pos + 3) & 3) * 2048),
          [sx] "s"(ox + ((pos + 3) & 3) * PAIR), [rs] "s"(rsv), [kcnt] "s"(kcnt), [c9] "s"(c9), [sg] "s"(sg),
          TOFF(0), TOFF(1), TOFF(2), TOFF(3), TOFF(4), TOFF(5), TOFF(6), TOFF(7)
        : "memory", "vcc", "scc", "s40", "s41", "s42", "s43", "s44", "s45", "s46", "s47",
          "v32", "v33", "v34", "v35", "v36", "v37", "v38", "v39", "v40", "v41", "v42", "v43", "v44", "v45", "v46", "v47", "v48", "v49", "v50", "v51", "v52", "v53", "v54", "v55", "v56", "v57", "v58", "v59", "v60", "v61", "v62", "v63", "v64", "v65", "v66", "v67");
}
__device__ __forceinline__ void oget(f32x16& O, LAS char* s, int par) {
    f32x2 t = *(volatile LAS f32x2*)(s + P_TAG + par * 8);
    f32x4 a = *(volatile LAS f32x4*)(s + par * 2048);
    f32x4 b = *(volatile LAS f32x4*)(s + par * 2048 + 1024);
    O[0] = a[0]; O[1] = a[1]; O[2] = a[2]; O[3] = a[3]; O[4] = b[0]; O[5] = b[1]; O[6] = b[2]; O[7] = b[3];
    O[8] = t[0]; O[9] = t[1];
}
__device__ __forceinline__ int lane_id() {
    int l; asm volatile("v_mbcnt_lo_u32_b32 %0, -1, 0\n\tv_mbcnt_hi_u32_b32 %0, -1, %0" : "=v"(l)); return l;
}

__device__ __forceinline__ void slow_tile(f32x16& O, const bf16x8& Q, __amdgpu_buffer_rsrc_t rs, int l) {
    const f32x16 z = {0};
    float m = -__builtin_inff();
#pragma unroll 1
    for (int t = 0; t < 64; ++t) {
        bf16x8 K = __builtin_bit_cast(bf16x8, __builtin_amdgcn_raw_buffer_load_b128(rs, W_K + t * 1024 + l * 16, 0, 16));
        f32x16 S = mfma32(K, Q, z);
#pragma unroll
        for (int r = 0; r < 16; ++r) m = fmaxf(m, S[r]);
    }
    m = fmaxf(m, __shfl_xor(m, 32));
    f32x16 C;
#pragma unroll
    for (int r = 0; r < 16; ++r) C[r] = -m;
    O = z;
    const unsigned one = ((l & 27) == 16) ? 0x3f803f80u : 0u;
#pragma unroll 1
    for (int t = 0; t < 64; ++t) {
        bf16x8 K = __builtin_bit_cast(bf16x8, __builtin_amdgcn_raw_buffer_load_b128(rs, W_K + t * 1024 + l * 16, 0, 16));
        v4u v0 = {one, one, one, one}, v1 = v0;
        if ((l & 16) == 0) {
            const int vo = W_V + t * 1024 + (l >> 5) * 256 + (l & 15) * 16;
            v0 = __builtin_amdgcn_raw_buffer_load_b128(rs, vo, 0, 16);
            v1 = __builtin_amdgcn_raw_buffer_load_b128(rs, vo + 512, 0, 16);
        }
        f32x16 S = mfma32(K, Q, C);
#pragma unroll
        for (int r = 0; r < 16; ++r) EX(S, r);
        O = pv(__builtin_bit_cast(bf16x8, v0), lo8(S), O, 0xDDDD);
        O = pv(__builtin_bit_cast(bf16x8, v1), hi8(S), O, 0xDDDD);
    }
}
__device__ __forceinline__ bool unsafe_sum(const f32x16& O) {
    float a = fabsf(O[0]);
#pragma unroll
    for (int r = 1; r < 9; ++r) a += fabsf(O[r]);
    return !(O[8] >= 0x1p-80f) || !(a < 0x1p100f);
}

__device__ __forceinline__ unsigned umax3(unsigned a, unsigned b, unsigned c) { return max(max(a, b), c); }
__device__ __forceinline__ unsigned umax4(unsigned m, const v4u& v) { return umax3(umax3(m, v[0], v[1]), v[2], v[3]); }
__device__ __forceinline__ void poll_frags(__amdgpu_buffer_rsrc_t rs, int base, int l) {
    const int off = base + (l & 15) * 1024 + 1020;
#pragma unroll 1
    for (unsigned s = 0; s < SPIN_CAP; ++s) {
        const unsigned v = __builtin_amdgcn_raw_buffer_load_b32(rs, off, 0, 16);
        if (__builtin_amdgcn_ballot_w64(v == SENT) == 0) break;
        __builtin_amdgcn_s_sleep(V_SL1);
        CFENCE();
    }
    CFENCE();
}
template <int V> struct IC { static constexpr int value = V; };
template <int N, int I = 0, class F>
__device__ __forceinline__ void static_for(F&& f) { if constexpr (I < N) { f(IC<I>{}); static_for<N, I + 1>(f); } }
template <bool TR>
__device__ __forceinline__ void bg_gemm(f32x16 (&t)[4], v4u w, unsigned src, unsigned ring, LAS char* r0, LAS char* r1, LAS char* xb, float init, int rq) {
    unsigned wb[4]; LAS char* xq[4];
#pragma unroll
    for (int i = 0; i < 4; ++i) { wb[i] = ((i + rq) & 3) * 393216; xq[i] = xb + ((i + rq) & 3) * 256; }
#define BG_MM(d, x, f) do { d = TR ? mfma32(f, x, s ? d : ini) : mfma32(x, f, s ? d : ini); PIN(d); } while (0)
    static_for<7>([&](auto S) { constexpr int s = S.value; glds_pair<s * 49152, 24576, s * 2048>(src, w, wb[0], ring); });
    f32x16 ini = (f32x16)init;
    asm volatile("" : "+v"(ini));
    wait_vm<12>();
    bf16x8 f0 = ldtr(r0, 0, 512), f1 = ldtr(r1, 0, 512), x0 = ld128(xq[0], 0), x1 = ld128(xq[0], 32 * XROW);
    static_for<32>([&](auto S) {
        constexpr int s = S.value;
        bf16x8 n0 = f0, n1 = f1, y0 = x0, y1 = x1;
        const int so = ((s + 1) % 7) * 2048;
        if (s <= 25) wait_vm<10>(); else if (s == 26) wait_vm<8>(); else if (s == 27) wait_vm<6>(); else if (s == 28) wait_vm<4>();
        else if (s == 29) wait_vm<2>(); else if (s == 30) wait_vm<0>();
        SB();
        BG_MM(t[0], x0, f0);
        if (s + 1 < 32) n0 = ldtr(r0, so, so + 512);
        SB(); GAP();
        BG_MM(t[2], x1, f0);
        if (s + 1 < 32) n1 = ldtr(r1, so, so + 512);
        SB(); GAP();
        BG_MM(t[1], x0, f1);
        if constexpr (s + 1 < 32) { y0 = ld128(xq[(s + 1) >> 3], ((s + 1) & 7) * 32); y1 = ld128(xq[(s + 1) >> 3], 32 * XROW + ((s + 1) & 7) * 32); }
        SB(); GAP();
        BG_MM(t[3], x1, f1);
        if constexpr (s + 7 < 32) glds_pair<((s + 7) & 7) * 49152, 24576, (s % 7) * 2048>(src, w, wb[(s + 7) >> 3], ring); else GAP();
        SB();
        f0 = n0; f1 = n1; x0 = y0; x1 = y1;
    });
#undef BG_MM
}
#define HORD(i) ((((i) & 7) << 1) | ((i) >> 3))
template <int N>
__device__ __forceinline__ unsigned proj_round(f32x16& c0, f32x16& c1, LAS char* wa, LAS char* wb, const bf16x8* of) {
    bf16x8 fa[2], fb[2];
    const f32x16 z = {0};
    unsigned m = 0;
    fa[0] = ldtr(wa, 0, 1024); fb[0] = ldtr(wb, 0, 1024);
#pragma unroll
    for (int i = 0; i < N; ++i) {
        if (i + 1 < N) {
            fb[(i + 1) & 1] = ldtr(wb, (i + 1) * 2048, (i + 1) * 2048 + 1024);
            fa[(i + 1) & 1] = ldtr(wa, (i + 1) * 2048, (i + 1) * 2048 + 1024);
        }
        SB();
        c1 = mfma32(fb[i & 1], of[i], i ? c1 : z);
        c0 = mfma32(fa[i & 1], of[i], i ? c0 : z);
        m = umax4(m, __builtin_bit_cast(v4u, of[i]));
        asm volatile("" : "+v"(c0), "+v"(c1), "+v"(m));
        SB();
    }
    return m;
}

__global__ __launch_bounds__(768, 3) void fused_kernel(Params p) {
    __shared__ __attribute__((aligned(1024))) char smem[L_TOTAL];
    LAS char* sm = (LAS char*)smem;
    const unsigned sm0 = (unsigned)(uintptr_t)sm;
    asm volatile("" :: "s"(p.x), "s"(p.wqkv), "s"(p.woa), "s"(p.wq), "s"(p.wk), "s"(p.wv), "s"(p.wob), "s"(p.gw), "s"(p.out), "s"(p.ws));
    int w = __builtin_amdgcn_readfirstlane(threadIdx.x >> 6);
    const int b = blockIdx.x, rk = b >> 3;
    const int hg0 = ((b & 7) % (8 / NHG)) * NHG;
    const int qt = ((b & 7) / (8 / NHG)) * MAPQ + (rk & (MAPQ - 1)), hg = hg0 + (int)((unsigned)rk / MAPQ);
    char* ws = p.ws;
    Stamps st = {};
    STAMP(0);
    const int rq = rk & 3;
#define ROTP (16 * rq)
    const int r0 = (hg & 3) * 16;
    const char* xt = p.x + qt * 65536;
    const __amdgpu_buffer_rsrc_t rs = __builtin_amdgcn_make_buffer_rsrc(ws, 0, W_END, 0x00020000);
    const v4u rsv = {(unsigned)(uintptr_t)ws, (unsigned)((uintptr_t)ws >> 32), (unsigned)W_END, 0x00020000u};
    LAS unsigned* cnt = (LAS unsigned*)(sm + L_CNT);
    const int l0 = threadIdx.x & 63;
#define ROLE_FENCE() asm volatile("" : "+s"(w))
    u64 e0;
    {
        const int l = l0;
        const unsigned l16 = l * 16;
        if (w < 8) {
            const char* wkv = ((hg >> 2) ? p.wv : p.wk) + w * 2048;
            const char* xr = xt + (r0 + 2 * w) * 1024;
            const unsigned dx = sm0 + L_X + (r0 + 2 * w) * XROW, dw = sm0 + L_WKV + w * 2048;
            glds16(l16, xr, dx);
            glds16(l16, wkv, dw);
            glds16(l16, xr + 1024, dx + XROW);
            glds16(l16, wkv + 1024, dw + 1024);
            wait_vm<0>();
        } else {
            SETPRIO(3);
            if (w == 11) {
                const char* pc;
                asm volatile("s_getpc_b64 %0" : "=s"(pc));
                glds4(min((unsigned)(rk + 32 * l), CODE_LINES - 1) * 128, pc - CODE_BACK, sm0 + L_DUMP);
                if (l < 16) *(LAS v4u*)(sm + L_CNT + l16) = (v4u){0u, 0u, 0u, 0u};
            }
            if (w == 9) glds16(l16, p.gw, sm0 + L_GW);
            if (w == 10 || (w == 9 && NHG == 8)) {
                __builtin_amdgcn_s_sleep(V_TQ);
                glds4((16 * rk + (((w - 9) * 64 + l) / NHG & 15)) * 1024 + (hg0 + l % NHG) * 128, p.wq, sm0 + L_DUMP);
            }
        }
        asm volatile("global_load_dwordx2 %0, %1, %2 sc1" : "=v"(e0) : "v"(0u), "s"(ws + W_E) : "memory");
        bar();
        STAMP(1);
        ROLE_FENCE();
        if (w < 8) {
#pragma unroll
            for (int i = 1; i < 4; ++i) { const int rr = ((r0 + 16 * i) & 63) + w; glds_run<2, 8192, 8 * XROW>(l16, make_rs(xt, 65536), rr * 1024, sm0 + L_X + rr * XROW); }
        } else {
            const int j = w - 8, g4 = l >> 4, q4 = (l >> 2) & 3, p4 = l & 3;
            LAS char* xk = sm + L_X + (r0 + (l & 15)) * XROW + g4 * 16 + j * 256;
            LAS char* wa = sm + L_WKV + (8 * g4 + q4) * 32 + p4 * 8 + j * 4096;
            bf16x8 xf[4], wf[4];
#pragma unroll
            for (int s = 0; s < 4; ++s) { xf[s] = ld128(xk, s * 64); wf[s] = ldtr(wa, s * 1024, s * 1024 + 128); }
            SB();
            f32x4 c0 = {0};
            if (hg >> 2) {
#pragma unroll
                for (int s = 0; s < 4; ++s) c0 = __builtin_amdgcn_mfma_f32_16x16x32_bf16(xf[s], wf[s], c0, 0, 0, 0);
            } else {
#pragma unroll
                for (int s = 0; s < 4; ++s) c0 = __builtin_amdgcn_mfma_f32_16x16x32_bf16(wf[s], xf[s], c0, 0, 0, 0);
            }
            LAS char* part = sm + L_OS0 + 16384 + l16;
            if (j != 0) {
                *(LAS f32x4*)(part + j * 1024) = c0;
                CFENCE();
                lds_arrive(sm0 + L_CNT + 44);
            } else {
                const int kg = qt * 64 + r0;
                const int off = (hg >> 2) ? W_V + (kg >> 4) * 512 + (g4 >> 1) * 256 + (l & 15) * 16 + (g4 & 1) * 8
                                          : W_K + (kg >> 5) * 1024 + (g4 & 1) * 512 + ((kg & 31) + (l & 15)) * 16 + (g4 >> 1) * 8;
                poll_lds_ge(cnt + 11, 3u);
                c0 = (c0 + *(LAS f32x4*)(part + 1024)) + (*(LAS f32x4*)(part + 2048) + *(LAS f32x4*)(part + 3072));
                v2u pv2 = {pk2(c0[0], c0[1]), pk2(c0[2], c0[3])};
                __builtin_amdgcn_raw_buffer_store_b64(pv2, rs, off, 0, 16);
            }
        }
        bar();
        STAMP(23);
        ROLE_FENCE();
    }
    {
        const int l = l0;
        if (w < 8) {
            const unsigned wsrc = (l >> 3) * 1024 + hg * 128 + (((l & 7) ^ ((l >> 2) & 4)) * 16);
            glds_run<8, 65536, 8192>(wsrc, make_rs(p.wq, 524288), w * 8192, sm0 + L_WQ + w * 1024);
        } else if (w == 8) {
            asm volatile("s_waitcnt vmcnt(1)" : "+v"(e0) :: "memory");
            const u64 t = e0 * 6364136223846793005ull + 1442695040888963407ull;
            if (l == 0) *(LAS u64*)(sm + L_TAG) = t;
            wait_vm<0>();
            if (l == 0) __hip_atomic_store((gu64*)(ws + W_F1) + qt * 8 + hg, t, RLX_AGENT);
            STAMP(2);
        } else if (w == 10) {
            const char* a = p.woa + (l & 7) * 65536;
            if (l >= 16) a = p.wob + (l & 7) * 65536;
            if (l >= 24) a = p.wqkv + (l - 24) * 65536;
            if (l >= 48) a = ws + (l - 48) * 524288 + 393216 + b * 128;
            if (l == 48) a = p.out + qt * 65536 + hg * 128;
            asm volatile("s_mov_b32 m0, %1\n\ts_nop 0\n\tglobal_load_lds_dword %0, off" :: "v"(a), "s"(sm0 + L_DUMP) : "memory");
        }
        if (w == 10) wait_vm<1>(); else if (w != 8) wait_vm<0>();
        asm volatile("" :: "v"(e0));
        bar();
        STAMP(3);
        ROLE_FENCE();
    }

    if (w < 8) {
        KV kv;
        f32x16 O0, O1;
        unsigned vb;
        {
            const int l = l0;
            const unsigned l16 = l * 16;
            const u64 tag = *(LAS u64*)(sm + L_TAG);
            const unsigned ko = W_K + w * 8192 + l16, vo = W_V + w * 8192 + ((l >> 4) & 1) * 512 + (l >> 5) * 256 + (l & 15) * 16;
            vb = (unsigned)__builtin_amdgcn_readfirstlane((unsigned)tag);
            if (V_PD) __builtin_amdgcn_s_sleep(V_PD);
            SETPRIO(3);
            poll_flags(ws + W_F1 + w * 256, tag, l);
            STAMP(14);
            if (w >= 4) {
                LAS char* xg = sm + L_X + (16 * (w & 3) + (l & 15)) * XROW + (l >> 4) * 16;
                LAS char* ga = sm + L_GW + (l >> 4) * 16;
                f32x4 c0 = {0}, c1 = {0};
#pragma unroll
                for (int hh = 0; hh < 2; ++hh) {
                    bf16x8 xf[8], gf[8];
#pragma unroll
                    for (int s = 0; s < 8; ++s) { xf[s] = ld128(xg, (hh * 8 + s) * 64); gf[s] = ld128(ga, (hh * 8 + s) * 64); }
                    SB();
#pragma unroll
                    for (int s = 0; s < 8; s += 2) {
                        c0 = __builtin_amdgcn_mfma_f32_16x16x32_bf16(gf[s], xf[s], c0, 0, 0, 0);
                        c1 = __builtin_amdgcn_mfma_f32_16x16x32_bf16(gf[s + 1], xf[s + 1], c1, 0, 0, 0);
                    }
                    SB();
                }
                if (l < 16) *(LAS float*)(sm + L_G + (16 * (w & 3) + l) * 4) = __builtin_amdgcn_rcpf(1.f + __expf(-(c0[0] + c1[0])));
            }
            if (w >= 4) poll_lds_ge<1>(cnt + 18, 4u);
            load_kv_first(kv, O0, O1, rsv, ko, vo, ((l & 11) == 0) ? 0x3f803f80u : 0u, V_ROT, sm0 + L_CNT + 72);
            STAMP(7);
            poll_lds_ge(cnt + 16, 4u);
            STAMP(4);
            attention(kv, O0, O1, l16, sm0 + ((w & 4) ? L_OS1 : L_OS0), sm0 + L_OS0, sm0 + L_Q, w, vb, (unsigned)(w & 4), rsv, V_ROT, sm0 + L_CNT + 72, sm0 + L_CNT + 36,
                      sm0 + L_G + (((w + 3) & 3) >> 1) * 128, st);
        }
        STAMP(8);
        const int l = lane_id();
        float gate = O0[14];
        if (__builtin_expect(__builtin_amdgcn_ballot_w64(__float_as_uint(O0[15]) == 0u) != 0, 0)) {
            poll_lds_ge(cnt + 9, 1u);
            gate = *(LAS float*)(sm + L_G + (32 * (((w + 3) & 3) >> 1) + (l & 31)) * 4);
        }
        const int pr = (w + 3) & 3;
        const int par = w >> 2;
        if (w < 4) {
            LAS char* own = sm + L_OS0 + pr * PAIR + l * 16;
            *(volatile LAS f32x4*)(own + 2048) = __builtin_shufflevector(O1, O1, 0, 1, 2, 3);
            *(volatile LAS f32x4*)(own + 3072) = __builtin_shufflevector(O1, O1, 4, 5, 6, 7);
            *(volatile LAS f32x2*)(own + P_TAG + 8) = (f32x2){O1[8], __uint_as_float(vb + 4u)};
            oget(O1, own, 0);
#pragma unroll 1
            for (unsigned sp = 0; sp < SPIN_CAP; ++sp) {
                oget(O0, sm + L_OS1 + pr * PAIR + l * 16, 0);
                if (__builtin_amdgcn_ballot_w64(__float_as_uint(O0[9]) != vb + 4u) == 0) break;
                __builtin_amdgcn_s_sleep(6);
            }
        }
#pragma unroll
        for (int r = 0; r < 9; ++r) O1[r] += O0[r];
        const int c = 2 * pr + par;
        if (__builtin_expect(__builtin_amdgcn_ballot_w64(unsafe_sum(O1)) != 0, 0)) slow_tile(O1, ld128(sm, L_Q + c * 1024 + l * 16), rs, l);
        const float sc = (1.f - gate) * __builtin_amdgcn_rcpf(O1[8]);
        bf16x8 ob = pack8(O1[0] * sc, O1[1] * sc, O1[2] * sc, O1[3] * sc, O1[4] * sc, O1[5] * sc, O1[6] * sc, O1[7] * sc);
        __builtin_amdgcn_raw_buffer_store_b128(__builtin_bit_cast(v4u, ob), rs,
                                               W_OB + ((qt * 2 + (c >> 2)) * 32 + hg * 4 + (c & 3)) * 1024 + l * 16, 0, 16);
        __builtin_amdgcn_s_barrier();
        if (b == 0 && w == 4 && l == 0) __hip_atomic_store((gu64*)(ws + W_E), *(LAS u64*)(sm + L_TAG), RLX_AGENT);
        STAMP(9);
        STAMP_FLUSH();
        return;
    }

    const int l = l0;
    const unsigned l16 = l * 16;
    const int j = w - 8, rh = j & 1, hp = j >> 1;
    const int g4 = l >> 4, q4 = (l >> 2) & 3, p4 = l & 3;
    const u64 tag = *(LAS u64*)(sm + L_TAG);
    LAS char* xa = sm + L_X + (32 * rh + (l & 31)) * XROW + (l >> 5) * 16;
    const int qchunk = (((4 * hp + 2 * (g4 & 1) + (p4 >> 1)) ^ ((q4 & 2) << 1)) * 16) + (p4 & 1) * 8;
    {
        LAS char* wa = sm + L_WQ + (8 * (g4 >> 1) + q4) * 128 + qchunk;
        f32x16 c = {0};
        bf16x8 wf[5], xf[5];
#pragma unroll
        for (int s = 0; s < 4; ++s) { wf[s] = ldtr(wa, s * 2048, s * 2048 + 512); xf[s] = ld128(xa, s * 32); }
#pragma unroll
        for (int s = 0; s < 32; ++s) {
            if (s + 4 < 32) { wf[(s + 4) % 5] = ldtr(wa, (s + 4) * 2048, (s + 4) * 2048 + 512); xf[(s + 4) % 5] = ld128(xa, (s + 4) * 32); }
            SB();
            c = mfma32(wf[s % 5], xf[s % 5], c);
            SB();
        }
        c *= 0.25f * 1.44269504088896341f;
        LAS char* qd = sm + L_Q + (rh * 4 + 2 * hp) * 1024 + l16;
        *(LAS bf16x8*)qd = pack8(c[0], c[1], c[2], c[3], c[4], c[5], c[6], c[7]);
        *(LAS bf16x8*)(qd + 1024) = pack8(c[8], c[9], c[10], c[11], c[12], c[13], c[14], c[15]);
        STAMP(21);
    }
    wait_lgkm0();
    lds_arrive(sm0 + L_CNT + 64);
    bf16x8 pq[4];
    {
        LAS char* x0a = sm + L_X + (l & 31) * XROW + (l >> 5) * 16;
        f32x16 t[4];
        LAS char* xch = sm + L_RING + l16;
        if (j < 3) {
            const unsigned src = (l >> 3) * 3072 + (j == 2 ? 0 : 1024 + j * 1024) + hg * 128 + (((l & 7) ^ ((l >> 2) & 4)) * 16);
            const unsigned ring = sm0 + L_RING + j * RING_SZ;
            const int q0 = (((2 * (g4 & 1) + (p4 >> 1)) ^ ((q4 & 2) << 1)) * 16) + (p4 & 1) * 8;
            LAS char* r0a = sm + L_RING + j * RING_SZ + (8 * (g4 >> 1) + q4) * 128 + q0;
            LAS char* r1a = sm + L_RING + j * RING_SZ + (8 * (g4 >> 1) + q4) * 128 + (q0 ^ 64);
            poll_lds_ge(cnt + 16, 4u);
            poll_lds_ge<2>(cnt + 18, V_BGW);
            STAMP(4);
            if (j == 2) bg_gemm<true>(t, make_rs(p.wqkv, 1572864), src, ring, r0a, r1a, x0a, PHI_EPS, rq);
            else bg_gemm<false>(t, make_rs(p.wqkv, 1572864), src, ring, r0a, r1a, x0a, j == 0 ? PHI_EPS : 0.f, rq);
            STAMP(5);
            wait_lgkm0();
            lds_arrive(sm0 + L_CNT + 68);
            poll_lds_ge<3>(cnt + 17, 3u);
            if (j == 1) {
#pragma unroll
                for (int i = 0; i < 4; ++i) {
                    const f32x16& T = t[(i & 1) * 2 + (i >> 1)];
                    *(LAS bf16x8*)(xch + 16384 + i * 2048) = pack8(T[0], T[1], T[2], T[3], T[4], T[5], T[6], T[7]);
                    *(LAS bf16x8*)(xch + 16384 + i * 2048 + 1024) = pack8(T[8], T[9], T[10], T[11], T[12], T[13], T[14], T[15]);
                }
            } else {
#pragma unroll
                for (int i = 0; i < 8; ++i) {
                    const f32x16& T = t[(i >> 2) * 2 + 1];
                    *(LAS f32x4*)(xch + j * 4096 + i * 1024) = (f32x4){T[4 * (i & 3)], T[4 * (i & 3) + 1], T[4 * (i & 3) + 2], T[4 * (i & 3) + 3]};
                }
                t[1] = t[2];
            }
            CFENCE();
            if (l == 0) __hip_atomic_store(cnt + 12 + j, 1u, RLX_WG);
        } else {
            const v4u sv = {SENT, SENT, SENT, SENT};
            const unsigned s1 = W_KVP + (hg * 32 + qt) * 8192, s2 = (qt * 64 + hg * 4) * 1024;
            poll_lds_ge<3>(cnt + 18, 8u);
            STAMP(14);
#pragma unroll
            for (int s = 0; s < 25; ++s) {
                if (s < 8) sent1(sv, rsv, l16, s1 + s * 1024);
                else if (s < 24) sent1(sv, rsv, l16, ((s & 8) ? W_OA : W_OB) + s2 + ((s >> 2) & 1) * 32768 + (s & 3) * 1024);
                else sent1(sv, rsv, (l & 15) * 16, W_KVS + hg * 8192 + qt * 256);
                if (s % 4 == 2 && s / 4 >= 1 && s / 4 <= NHG / 2)
                    glds4((16 * rk + ((((s / 4 - 1) >> 1) * 64 + l) / NHG & 15)) * 1024 + (hg0 + l % NHG) * 128, ((s / 4) & 1) ? p.woa : p.wob, sm0 + L_DUMP);
                __builtin_amdgcn_s_sleep(V_SP);
            }
            wait_vm<0>();
            if (l == 0) { __hip_atomic_store((gu64*)(ws + W_F6) + (qt * 8 + hg) * 16, tag, RLX_AGENT); __hip_atomic_store(cnt + 9, 1u, RLX_WG); }
            STAMP(22);
            const gu64* f6 = (const gu64*)(ws + W_F6) + (l < 32 ? l * 8 + hg : qt * 8 + (l & 7)) * 16;
#pragma unroll 1
            for (unsigned sp = 0; sp < SPIN_CAP; ++sp) {
                __builtin_amdgcn_s_sleep(12);
                if (__builtin_amdgcn_ballot_w64(__hip_atomic_load(f6, RLX_AGENT) != tag) == 0) break;
            }
            CFENCE();
            if (l == 0) __hip_atomic_store(cnt + 9, 2u, RLX_WG);
            STAMP(5);
        }
        if (j & 1) {
            const int from = j == 1 ? 2 : 0;
            poll_lds_ge<4>(cnt + 12 + from, 1u);
#pragma unroll
            for (int i = 0; i < 8; ++i) {
                const f32x4 u = *(LAS f32x4*)(xch + from * 4096 + i * 1024);
#pragma unroll
                for (int k = 0; k < 4; ++k) t[i >> 2][4 * (i & 3) + k] = u[k];
            }
        }
#pragma unroll
        for (int i = 0; i < 4; ++i) {
            const f32x16& T = t[i >> 1];
            const int o = (i & 1) * 8;
            pq[i] = __builtin_bit_cast(bf16x8, (v4u){phi2(T[o], T[o + 1]), phi2(T[o + 2], T[o + 3]), phi2(T[o + 4], T[o + 5]), phi2(T[o + 6], T[o + 7])});
        }
        if (j == 0 || j == 3) {
            const int pr = j & 1;
            poll_lds_ge<3>(cnt + 13, 1u);
            const bf16x8 ones = __builtin_bit_cast(bf16x8, (v4u){0x3f803f80u, 0x3f803f80u, 0x3f803f80u, 0x3f803f80u});
            f32x16 R = {0}, Z = {0};
#pragma unroll
            for (int i = 0; i < 4; ++i) { R = mfma32(pq[i], ld128(xch, 16384 + pr * 4096 + i * 1024), R); Z = mfma32(pq[i], ones, Z); }
            poll_lds_ge(cnt + 9, 1u);
            const bool lo = (l & 16) == 0;
            const int off = W_KVP + (hg * 32 + qt) * 8192 + pr * 4096 + l16;
#pragma unroll
            for (int i = 0; i < 4; ++i) {
                f32x4 v;
#pragma unroll
                for (int k = 0; k < 4; ++k) v[k] = (lo == (i < 2)) ? R[4 * i + k] : Z[4 * i + k];
                __builtin_amdgcn_raw_buffer_store_b128(__builtin_bit_cast(v4u, v), rs, off + i * 1024, 0, 16);
            }
        }
        wait_lgkm0();
        lds_arrive(sm0 + L_CNT);
        STAMP(6);
    }
    const unsigned wsrc = (l >> 3) * 1024 + hg * 128 + (((l & 7) ^ ((l >> 2) & 4)) * 16);
    const int trow = (4 * (g4 >> 1) + q4) * 128;
    const v4u rsa = make_rs(p.woa, 524288), rsb = make_rs(p.wob, 524288);
    bf16x8 wl0[5], wl1[5];
#define STAGE_WL() do { poll_lds_ge<4>(cnt + 0, 4u); \
        glds_run<10, 8192, 1024>(wsrc, rsa, (hp * 32 + 22) * 8192, sm0 + L_RING + j * 10240); } while (0)
#define READ_WL() do { \
        LAS char* a_ = sm + L_RING + j * 10240 + trow + qchunk; LAS char* b_ = sm + L_RING + j * 10240 + trow + (qchunk ^ 64); \
        _Pragma("unroll") for (int i = 0; i < 5; ++i) { wl0[i] = ldtr(a_, i * 2048, i * 2048 + 1024); wl1[i] = ldtr(b_, i * 2048, i * 2048 + 1024); } \
        _Pragma("unroll") for (int i = 0; i < 5; ++i) asm volatile("" : "+v"(wl0[i]), "+v"(wl1[i])); \
        wait_lgkm0(); lds_arrive(sm0 + L_CNT + 12); } while (0)
    if (j == 0) {
        STAGE_WL();
#pragma unroll
        for (int i = 0; i < 4; ++i) { const int u = (i + rq) & 3; glds_run<16, 8192, 1024>(wsrc, rsb, u * 131072, sm0 + u * 16384); if (i == 2) wait_vm<40>(); }
        READ_WL();
        poll_lds_ge<6>(cnt + 3, 4u);
#pragma unroll
        for (int i = 0; i < 4; ++i) { const int u = (i + rq) & 3; glds_run<11, 8192, 1024>(wsrc, rsa, ((u >> 1) * 32 + (u & 1) * 11) * 8192, sm0 + L_RING + u * 11264); }
        STAMP(7);
    } else if (j == 3) {
        float s;
#pragma unroll 1
        for (unsigned sp = 0; sp < SPIN_CAP; ++sp) {
            unsigned v[32];
#pragma unroll
            for (int i = 0; i < 32; ++i) v[i] = __builtin_amdgcn_raw_buffer_load_b32(rs, qt * 256 + l * 4, W_KVP + (hg * 32 + i) * 8192, 16);
            SB();
            unsigned m = 0, m2 = 0;
            f32x2 a = {0.f, 0.f}, c = {0.f, 0.f};
#pragma unroll
            for (int i = 0; i < 32; i += 4) {
                m = umax3(m, v[i], v[i + 1]); m2 = umax3(m2, v[i + 2], v[i + 3]);
                a += (f32x2){__uint_as_float(v[i]), __uint_as_float(v[i + 1])}; c += (f32x2){__uint_as_float(v[i + 2]), __uint_as_float(v[i + 3])};
            }
            m = max(m, m2); a += c; s = a[0] + a[1];
            if (__builtin_amdgcn_ballot_w64(m == SENT) == 0) break;
            __builtin_amdgcn_s_sleep(4);
            CFENCE();
        }
        __builtin_amdgcn_raw_buffer_store_b32(__float_as_uint(s), rs, W_KVS + hg * 8192 + qt * 256 + l * 4, 0, 16);
        STAMP(7);
        STAGE_WL();
        wait_vm<0>();
        READ_WL();
    } else {
        const int pr = 2 - j;
        STAGE_WL();
        wait_vm<0>();
        READ_WL();
        poll_lds_ge<4>(cnt + 9, 2u);
        const float g0 = *(LAS float*)(sm + L_G + (l & 31) * 4), g1 = *(LAS float*)(sm + L_G + 128 + (l & 31) * 4);
        v4u ku[2], kw[2];
#pragma unroll 1
        for (unsigned sp = 0; sp < SPIN_CAP; ++sp) {
#pragma unroll
            for (int a = 0; a < 2; ++a) {
                ku[a] = __builtin_amdgcn_raw_buffer_load_b128(rs, l16, W_KVS + hg * 8192 + (2 * pr + a) * 2048, 16);
                kw[a] = __builtin_amdgcn_raw_buffer_load_b128(rs, l16, W_KVS + hg * 8192 + (2 * pr + a) * 2048 + 1024, 16);
            }
            SB();
            const unsigned m = max(umax4(umax4(0u, ku[0]), kw[0]), umax4(umax4(0u, ku[1]), kw[1]));
            if (__builtin_amdgcn_ballot_w64(m == SENT) == 0) break;
            __builtin_amdgcn_s_sleep(6);
            CFENCE();
        }
        STAMP(9);
#pragma unroll
        for (int a = 0; a < 2; ++a) {
            const f32x4 u = __builtin_bit_cast(f32x4, ku[a]), v = __builtin_bit_cast(f32x4, kw[a]);
            const bf16x8 kf = pack8(u[0], u[1], u[2], u[3], v[0], v[1], v[2], v[3]);
#pragma unroll
            for (int r2 = 0; r2 < 2; ++r2) {
                f32x16 z = {0};
                f32x16 d = mfma32(kf, pq[2 * r2 + a], z);
                const int n = a * 8;
                const float sc = (r2 ? g1 : g0) * __builtin_amdgcn_rcpf(d[8 - n] + 1e-6f);
                const f32x2 s2 = {sc, sc};
                const f32x2 o0 = (f32x2){d[n], d[n + 1]} * s2, o1 = (f32x2){d[n + 2], d[n + 3]} * s2, o2 = (f32x2){d[n + 4], d[n + 5]} * s2, o3 = (f32x2){d[n + 6], d[n + 7]} * s2;
                __builtin_amdgcn_raw_buffer_store_b128((v4u){pk2(o0[0], o0[1]), pk2(o1[0], o1[1]), pk2(o2[0], o2[1]), pk2(o3[0], o3[1])}, rs, l16,
                                                       W_OA + ((qt * 2 + r2) * 32 + hg * 4 + 2 * pr + a) * 1024, 16);
            }
        }
        STAMP(10);
    }
    const int ob = ((qt * 2 + rh) * 32 + 16 * hp) * 1024;
    f32x16 c0, c1;
    bf16x8 ofb[16];
    {
        bf16x8 of[16];
        poll_lds_ge<6>(cnt + 9, 2u);
        poll_frags(rs, W_OA + ob, l);
#pragma unroll 1
        for (unsigned sp = 0; sp < SPIN_CAP; ++sp) {
#pragma unroll
            for (int i = 0; i < 16; ++i) of[i] = __builtin_bit_cast(bf16x8, __builtin_amdgcn_raw_buffer_load_b128(rs, l16, W_OA + ob + i * 1024, 16));
            SB();
            unsigned m = 0;
#pragma unroll
            for (int i = 11; i < 16; ++i) m = umax4(m, __builtin_bit_cast(v4u, of[i]));
            if (__builtin_amdgcn_ballot_w64(m == SENT) == 0) break;
            CFENCE();
        }
        lds_arrive(sm0 + L_CNT + 12);
        STAMP(11);
        poll_lds_ge<6>(cnt + 3, 8u);
        __builtin_amdgcn_s_barrier();
        CFENCE();
        STAMP(13);
#pragma unroll 1
        for (unsigned sp = 0; sp < SPIN_CAP; ++sp) {
            const unsigned m = proj_round<11>(c0, c1, sm + L_RING + hp * 22528 + trow + qchunk, sm + L_RING + hp * 22528 + trow + (qchunk ^ 64), of);
            if (__builtin_expect(__builtin_amdgcn_ballot_w64(m == SENT) == 0, 1)) break;
            CFENCE();
#pragma unroll
            for (int i = 0; i < 11; ++i) of[i] = __builtin_bit_cast(bf16x8, __builtin_amdgcn_raw_buffer_load_b128(rs, l16, W_OA + ob + i * 1024, 16));
        }
#pragma unroll
        for (int i = 0; i < 16; ++i) ofb[i] = __builtin_bit_cast(bf16x8, __builtin_amdgcn_raw_buffer_load_b128(rs, l16, W_OB + ob + HORD(i) * 1024, 16));
        SB();
#pragma unroll
        for (int i = 0; i < 5; ++i) { c1 = mfma32(wl1[i], of[11 + i], c1); c0 = mfma32(wl0[i], of[11 + i], c0); }
        asm volatile("" : "+v"(c0), "+v"(c1));
        SB();
    }
    STAMP(12);
    {
        LAS char* wa = sm + hp * 32768 + trow + qchunk;
        LAS char* wb = sm + hp * 32768 + trow + (qchunk ^ 64);
        bf16x8 wf0[8], wf1[8];
#pragma unroll
        for (int i = 0; i < 4; ++i) { wf0[i] = ldtr(wa, HORD(i) * 2048, HORD(i) * 2048 + 1024); wf1[i] = ldtr(wb, HORD(i) * 2048, HORD(i) * 2048 + 1024); }
        SB();
#pragma unroll
        for (int gq = 0; gq < 4; ++gq) {
            if (gq < 3) {
#pragma unroll
                for (int i = 4; i < 8; ++i) {
                    const int k = 4 * gq + i;
                    wf0[k & 7] = ldtr(wa, HORD(k) * 2048, HORD(k) * 2048 + 1024); wf1[k & 7] = ldtr(wb, HORD(k) * 2048, HORD(k) * 2048 + 1024);
                }
            }
            unsigned m = 0;
#pragma unroll
            for (int i = 0; i < 4; ++i) m = umax4(m, __builtin_bit_cast(v4u, ofb[4 * gq + i]));
            if (__builtin_expect(__builtin_amdgcn_ballot_w64(m == SENT) != 0, 0)) {
#pragma unroll 1
                for (unsigned sp = 0; sp < SPIN_CAP; ++sp) {
                    CFENCE();
                    m = 0;
#pragma unroll
                    for (int i = 0; i < 4; ++i)
                        ofb[4 * gq + i] = __builtin_bit_cast(bf16x8, __builtin_amdgcn_raw_buffer_load_b128(rs, l16, W_OB + ob + HORD(4 * gq + i) * 1024, 16));
#pragma unroll
                    for (int i = 0; i < 4; ++i) m = umax4(m, __builtin_bit_cast(v4u, ofb[4 * gq + i]));
                    if (__builtin_amdgcn_ballot_w64(m == SENT) == 0) break;
                    __builtin_amdgcn_s_sleep(1);
                }
            }
#pragma unroll
            for (int i = 0; i < 4; ++i) {
                const int k = 4 * gq + i;
                c1 = mfma32(wf1[k & 7], ofb[k], c1); c0 = mfma32(wf0[k & 7], ofb[k], c0);
            }
            asm volatile("" : "+v"(c0), "+v"(c1));
            SB();
        }
    }
    LAS char* ex = sm + L_OS0;
    STAMP(15);
#pragma unroll
    for (int i = 0; i < 4; ++i)
        *(LAS f32x4*)(ex + j * 4096 + i * 1024 + l16) = (f32x4){c1[4 * i], c1[4 * i + 1], c1[4 * i + 2], c1[4 * i + 3]};
    CFENCE();
    if (l == 0) __hip_atomic_store(cnt + 4 + j, 1u, RLX_WG);
    poll_lds_ge(cnt + 4 + (j ^ 2), 1u);
    unsigned o2[8];
#pragma unroll
    for (int i = 0; i < 4; ++i) {
        f32x4 t = *(LAS f32x4*)(ex + (j ^ 2) * 4096 + i * 1024 + l16);
        o2[2 * i] = pk2(c0[4 * i] + t[0], c0[4 * i + 1] + t[1]);
        o2[2 * i + 1] = pk2(c0[4 * i + 2] + t[2], c0[4 * i + 3] + t[3]);
    }
    const int l2 = lane_id();
    char* orow = p.out + (size_t)(64 * qt + 32 * rh) * 1024 + hg * 128 + hp * 64 + ((l2 & 31) * 1024 + (l2 >> 5) * 16);
#pragma unroll
    for (int k = 0; k < 2; ++k) {
        auto sx = __builtin_amdgcn_permlane32_swap(o2[4 * k], o2[4 * k + 2], false, false);
        auto sy = __builtin_amdgcn_permlane32_swap(o2[4 * k + 1], o2[4 * k + 3], false, false);
        *(GAS v4u*)(orow + k * 32) = (v4u){sx[0], sy[0], sx[1], sy[1]};
    }
    STAMP(20);
    STAMP_FLUSH();
}

extern "C" void kernel_launch(void* const* d_in, const int* in_sizes, int n_in, void* d_out, int out_size, void* d_ws,
                              size_t ws_size, hipStream_t stream) {
    Params p;
    p.x = (const char*)d_in[0]; p.wqkv = (const char*)d_in[1]; p.woa = (const char*)d_in[2];
    p.wq = (const char*)d_in[4]; p.wk = (const char*)d_in[5]; p.wv = (const char*)d_in[6];
    p.wob = (const char*)d_in[7]; p.gw = (const char*)d_in[8];
    p.out = (char*)d_out; p.ws = (char*)d_ws;
    fused_kernel<<<256, 768, 0, stream>>>(p);
}
```
